```python
import torch

def _softmax(x, axis=-1):
    return torch.softmax(x, dim=axis)

def _gelu(x):
    return torch.nn.functional.gelu(x, approximate='tanh')

def _silu(x):
    return torch.nn.functional.silu(x)

def _sigmoid(x):
    return torch.sigmoid(x)

def _layernorm(x, axis=-1, eps=1e-05):
    m = x.mean(dim=axis, keepdim=True)
    v = x.var(dim=axis, keepdim=True, unbiased=False)
    return (x - m) / torch.sqrt(v + eps)

def _scatter_add(tgt, idx, src, axis=0):
    return tgt.clone().index_add_(axis, idx, src)

def _segment_sum(src, seg, n):
    out = torch.zeros((n,) + src.shape[1:], dtype=src.dtype)
    return out.index_add_(0, seg, src)

def _pad(x, pw):
    flat = tuple((v for pair in reversed(pw) for v in pair))
    return torch.nn.functional.pad(x, flat)
kernel_name = 'adaLN_cond__sliding_block__dense_activation_relu__prompt'

def setup_inputs():

    _w = lambda a: (lambda t: t.to(torch.bfloat16) if t.is_floating_point() else t)(torch.as_tensor(a))

    def _A_setup():
        B, S, D = (1, 2048, 448)
        return {'x': _w((lambda *s: torch.randn(*s, dtype=torch.bfloat16) * 0.05)(B, S, D)), 'wqkv': _w((lambda *s: torch.randn(*s, dtype=torch.bfloat16) * 0.05)(D, 3 * D)), 'wo': _w((lambda *s: torch.randn(*s, dtype=torch.bfloat16) * 0.05)(D, D))}
    _A_in = _A_setup()

    def _B_setup():
        B, S, D, Dff = (1, 2048, 448, 1792)
        return {'x': _w((lambda *s: torch.randn(*s, dtype=torch.bfloat16) * 0.05)(B, S, D)), 'w1': _w((lambda *s: torch.randn(*s, dtype=torch.bfloat16) * 0.05)(D, Dff)), 'w2': _w((lambda *s: torch.randn(*s, dtype=torch.bfloat16) * 0.05)(Dff, D))}
    _B_in = _B_setup()
    return {'A_x': _A_in['x'], 'A_wqkv': _A_in['wqkv'], 'A_wo': _A_in['wo'], 'B_x': _B_in['x'], 'B_w1': _B_in['w1'], 'B_w2': _B_in['w2'], 'c': _w((lambda *s: torch.randn(*s, dtype=torch.bfloat16) * 0.1)(1, 448) * 8.0), 'w_mod': _w((lambda *s: torch.randn(*s, dtype=torch.bfloat16) * 0.1)(448, 6 * 448) * 448 ** (-0.5) * 8.0)}

def reference(A_x, A_wqkv, A_wo, B_x, B_w1, B_w2, c, w_mod):

    def _A_ref(x, wqkv, wo):
        B, S, D = x.shape
        H = 16
        Dh = D // H
        W = 256
        qkv = (lambda x, *d: x.permute(*d))((x @ wqkv).reshape(B, S, 3, H, Dh), 2, 0, 3, 1, 4)
        q, k, v = (qkv[0], qkv[1], qkv[2])
        scores = torch.einsum('bhqd,bhkd->bhqk', q, k) * Dh ** (-0.5)
        idx = torch.arange(S)
        mask = (idx[:, None] - idx[None, :] >= 0) & (idx[:, None] - idx[None, :] < W)
        scores = torch.where(mask[None, None], scores, -1000000000.0)
        p = _softmax(scores, axis=-1)
        o = torch.einsum('bhqk,bhkd->bhqd', p, v)
        return (lambda x, *d: x.permute(*d))(o, 0, 2, 1, 3).reshape(B, S, D) @ wo

    def _B_ref(x, w1, w2):
        h = torch.einsum('bsd,df->bsf', x, w1)
        zero = torch.zeros_like(h)
        h = torch.maximum(h, zero)
        return torch.einsum('bsf,fd->bsd', h, w2)
    x = A_x
    mod = (_silu(c) @ w_mod).reshape(c.shape[0], 6, 1, -1)
    sa, ca, ga, sb, cb, gb = (mod[:, 0].clone(), mod[:, 1].clone(), mod[:, 2].clone(), mod[:, 3].clone(), mod[:, 4].clone(), mod[:, 5].clone())
    h = _layernorm(x, axis=-1) * (1 + ca) + sa
    x = x + ga * _A_ref(h, A_wqkv, A_wo)
    h = _layernorm(x, axis=-1) * (1 + cb) + sb
    x = x + gb * _B_ref(h, B_w1, B_w2)
    return x
```

```cpp
#include <hip/hip_runtime.h>
#include <stdint.h>

typedef __attribute__((ext_vector_type(8))) __bf16 bf16x8;
typedef __attribute__((ext_vector_type(2))) __bf16 bf16x2;
typedef __attribute__((ext_vector_type(2))) float f32x2;
typedef __attribute__((ext_vector_type(4))) float f32x4;
typedef __attribute__((ext_vector_type(16))) float f32x16;
typedef __attribute__((ext_vector_type(4))) unsigned u32x4;
typedef __attribute__((ext_vector_type(2))) unsigned u32x2;
typedef unsigned short u16;

#define DI __device__ __forceinline__
#define MFMA(a, b, c) __builtin_amdgcn_mfma_f32_32x32x16_bf16(__builtin_bit_cast(bf16x8, (a)), __builtin_bit_cast(bf16x8, (b)), (c), 0, 0, 0)

constexpr int S_ = 2048, D_ = 448, DFF = 1792, NH = 16, DH = 28;
constexpr int KS_D = 28;
constexpr int KS_O = 32;
constexpr int KS_F = 112;

constexpr size_t OFF_FLAGS = 0;
constexpr size_t OFF_WQKV = 65536;
constexpr size_t OFF_WO = OFF_WQKV + 48 * 28 * 1024;
constexpr size_t OFF_W1 = OFF_WO + 14 * 32 * 1024;
constexpr size_t OFF_W2 = OFF_W1 + 56 * 28 * 1024;
constexpr size_t OFF_XF = OFF_W2 + 14 * 112 * 1024;
constexpr size_t OFF_ST1 = OFF_XF + 64 * 28 * 1024;
constexpr size_t OFF_GB1 = OFF_ST1 + 2048 * 8;
constexpr size_t OFF_GB2 = OFF_GB1 + 28 * 1536 * 8;
constexpr size_t OFF_GAGB = OFF_GB2 + 28 * 1792 * 8;
constexpr size_t OFF_Q = OFF_GAGB + 4096;
constexpr size_t OFF_K = OFF_Q + 16 * 64 * 2048;
constexpr size_t OFF_V = OFF_K + 16 * 64 * 2048;
constexpr size_t OFF_O = OFF_V + 16 * 64 * 2048;
constexpr size_t OFF_X1 = OFF_O + 64 * 32 * 1024;
constexpr size_t OFF_ST2 = OFF_X1 + 64 * 28 * 1024;
constexpr size_t OFF_H1 = OFF_ST2 + 2048 * 14 * 8;

DI float bf2f(u16 v) { return __uint_as_float(((unsigned)v) << 16); }
DI float bflo(unsigned v) { return __uint_as_float(v << 16); }
DI float bfhi(unsigned v) { return __uint_as_float(v & 0xffff0000u); }
DI unsigned pk2(float a, float b) {
    f32x2 v = {a, b};
    bf16x2 r = __builtin_convertvector(v, bf16x2);
    return __builtin_bit_cast(unsigned, r);
}
DI float rbf(float a) { return bflo(pk2(a, 0.f)); }
DI int kperm(int h, int j) { return 8 * (j >> 2) + 4 * h + (j & 3); }

struct Params {
    const u16* x; const u16* wqkv; const u16* wo; const u16* w1; const u16* w2; const u16* c; const u16* wmod;
    u16* out; char* ws;
};

DI void mod_slice(const Params& p, int c0, int c1, float* sm  ) {
    const int t = threadIdx.x, rg = t >> 2, pc = t & 3, w = t >> 6;
    const int col = (pc < 2 ? c0 : c1) + (pc & 1) * 8;
    u32x4 wv[7]; u16 cv[7];
#pragma unroll
    for (int i = 0; i < 7; i++) {
        const int row = rg + 64 * i;
        wv[i] = *(const u32x4*)(p.wmod + (size_t)row * 2688 + col);
        cv[i] = p.c[row];
    }
    float acc[8];
#pragma unroll
    for (int j = 0; j < 8; j++) acc[j] = 0.f;
#pragma unroll
    for (int i = 0; i < 7; i++) {
        float cf = bf2f(cv[i]);
        float s = rbf(cf / (1.f + __expf(-cf)));
#pragma unroll
        for (int j = 0; j < 4; j++) { acc[2 * j] += s * bflo(wv[i][j]); acc[2 * j + 1] += s * bfhi(wv[i][j]); }
    }
#pragma unroll
    for (int j = 0; j < 8; j++) {
        acc[j] += __shfl_xor(acc[j], 4); acc[j] += __shfl_xor(acc[j], 8);
        acc[j] += __shfl_xor(acc[j], 16); acc[j] += __shfl_xor(acc[j], 32);
    }
    if ((t & 63) < 4) {
#pragma unroll
        for (int j = 0; j < 8; j++) sm[32 + w * 32 + pc * 8 + j] = acc[j];
    }
    __syncthreads();
    if (t < 32) sm[t] = rbf(sm[32 + t] + sm[64 + t] + sm[96 + t] + sm[128 + t]);
    __syncthreads();
}

constexpr int NB_QKV = 28 * 4, NB_W1 = 28 * 2, NB_WO = 28, NB_W2 = 98, NB_X = 64, NB_G = 28;
constexpr int NB_PREP = NB_QKV + NB_W1 + NB_WO + NB_W2 + NB_X + NB_G;

__global__ __launch_bounds__(256) void k_prep(Params p) {
    __shared__ float sm[256];
    int b = blockIdx.x;
    const int t = threadIdx.x, lane = t & 63, w = t >> 6, c = lane & 31, h = lane >> 5;
    if (b < NB_QKV) {
        const int S = b >> 2, head = (b & 3) * 4 + w;
        u16 v[3][8];
#pragma unroll
        for (int tt = 0; tt < 3; tt++)
#pragma unroll
            for (int j = 0; j < 8; j++) {
                const int k = 16 * S + kperm(h, j);
                v[tt][j] = (c < DH) ? p.wqkv[(size_t)k * 1344 + tt * 448 + head * DH + c] : (u16)0;
            }
        mod_slice(p, 16 * S, 448 + 16 * S, sm);
        float g[8], sa[8];
#pragma unroll
        for (int j = 0; j < 8; j++) { const int kk = kperm(h, j); sa[j] = sm[kk]; g[j] = rbf(1.f + sm[16 + kk]); }
#pragma unroll
        for (int tt = 0; tt < 3; tt++) {
            u32x4 o; float G = 0.f, B = 0.f;
#pragma unroll
            for (int j = 0; j < 4; j++) {
                const float w0 = bf2f(v[tt][2 * j]), w1 = bf2f(v[tt][2 * j + 1]);
                o[j] = pk2(w0 * g[2 * j], w1 * g[2 * j + 1]);
                G += bflo(o[j]) + bfhi(o[j]);
                B += w0 * sa[2 * j] + w1 * sa[2 * j + 1];
            }
            const int nt = head * 3 + tt;
            *(u32x4*)(p.ws + OFF_WQKV + ((size_t)(nt * KS_D + S) * 64 + lane) * 16) = o;
            G += __shfl_xor(G, 32); B += __shfl_xor(B, 32);
            if (h == 0) *(f32x2*)(p.ws + OFF_GB1 + ((size_t)S * 1536 + nt * 32 + c) * 8) = f32x2{G, B};
        }
        return;
    }
    b -= NB_QKV;
    if (b < NB_W1) {
        const int S = b >> 1, nt0 = (b & 1) * 28 + w * 7;
        u16 v[7][8];
#pragma unroll
        for (int tt = 0; tt < 7; tt++)
#pragma unroll
            for (int j = 0; j < 8; j++) {
                const int k = 16 * S + kperm(h, j);
                v[tt][j] = p.w1[(size_t)k * DFF + (nt0 + tt) * 32 + c];
            }
        mod_slice(p, 1344 + 16 * S, 1792 + 16 * S, sm);
        float g[8], sa[8];
#pragma unroll
        for (int j = 0; j < 8; j++) { const int kk = kperm(h, j); sa[j] = sm[kk]; g[j] = rbf(1.f + sm[16 + kk]); }
#pragma unroll
        for (int tt = 0; tt < 7; tt++) {
            u32x4 o; float G = 0.f, B = 0.f;
#pragma unroll
            for (int j = 0; j < 4; j++) {
                const float w0 = bf2f(v[tt][2 * j]), w1 = bf2f(v[tt][2 * j + 1]);
                o[j] = pk2(w0 * g[2 * j], w1 * g[2 * j + 1]);
                G += bflo(o[j]) + bfhi(o[j]);
                B += w0 * sa[2 * j] + w1 * sa[2 * j + 1];
            }
            const int nt = nt0 + tt;
            *(u32x4*)(p.ws + OFF_W1 + ((size_t)(nt * KS_D + S) * 64 + lane) * 16) = o;
            G += __shfl_xor(G, 32); B += __shfl_xor(B, 32);
            if (h == 0) *(f32x2*)(p.ws + OFF_GB2 + ((size_t)S * 1792 + nt * 32 + c) * 8) = f32x2{G, B};
        }
        return;
    }
    b -= NB_W1;
    if (b < NB_WO) {
#pragma unroll
        for (int i = 0; i < 4; i++) {
            const int f = b * 16 + w * 4 + i, Sp = f / 14, nt = f % 14, head = Sp >> 1, s = Sp & 1;
            u16 v[8];
#pragma unroll
            for (int j = 0; j < 8; j++) {
                const int d = 16 * s + kperm(h, j);
                v[j] = (d < DH) ? p.wo[(size_t)(head * DH + d) * D_ + nt * 32 + c] : (u16)0;
            }
            u32x4 o;
#pragma unroll
            for (int j = 0; j < 4; j++) o[j] = (unsigned)v[2 * j] | ((unsigned)v[2 * j + 1] << 16);
            *(u32x4*)(p.ws + OFF_WO + ((size_t)(nt * KS_O + Sp) * 64 + lane) * 16) = o;
        }
        return;
    }
    b -= NB_WO;
    if (b < NB_W2) {
#pragma unroll
        for (int i = 0; i < 4; i++) {
            const int f = b * 16 + w * 4 + i, S = f / 14, nt = f % 14;
            u16 v[8];
#pragma unroll
            for (int j = 0; j < 8; j++) v[j] = p.w2[(size_t)(16 * S + kperm(h, j)) * D_ + nt * 32 + c];
            u32x4 o;
#pragma unroll
            for (int j = 0; j < 4; j++) o[j] = (unsigned)v[2 * j] | ((unsigned)v[2 * j + 1] << 16);
            *(u32x4*)(p.ws + OFF_W2 + ((size_t)(nt * KS_F + S) * 64 + lane) * 16) = o;
        }
        return;
    }
    b -= NB_W2;
    if (b < NB_X) {
        const int mt = b;
        const u16* row = p.x + (size_t)(mt * 32 + c) * D_;
        u32x4 v[7];
        float s = 0.f;
#pragma unroll
        for (int i = 0; i < 7; i++) {
            const int S = w * 7 + i;
            u32x2 a = *(const u32x2*)(row + 16 * S + 4 * h);
            u32x2 bb = *(const u32x2*)(row + 16 * S + 8 + 4 * h);
            v[i] = u32x4{a[0], a[1], bb[0], bb[1]};
        }
#pragma unroll
        for (int i = 0; i < 7; i++) {
            *(u32x4*)(p.ws + OFF_XF + ((size_t)(mt * KS_D + w * 7 + i) * 64 + lane) * 16) = v[i];
#pragma unroll
            for (int j = 0; j < 4; j++) s += bflo(v[i][j]) + bfhi(v[i][j]);
        }
        s += __shfl_xor(s, 32);
        if (h == 0) sm[w * 32 + c] = s;
        __syncthreads();
        const float mean = (sm[c] + sm[32 + c] + sm[64 + c] + sm[96 + c]) * (1.f / D_);
        float q = 0.f;
#pragma unroll
        for (int i = 0; i < 7; i++)
#pragma unroll
            for (int j = 0; j < 4; j++) { float a = bflo(v[i][j]) - mean, bb = bfhi(v[i][j]) - mean; q += a * a + bb * bb; }
        q += __shfl_xor(q, 32);
        if (h == 0) sm[128 + w * 32 + c] = q;
        __syncthreads();
        if (t < 32) {
            const float var = (sm[128 + c] + sm[160 + c] + sm[192 + c] + sm[224 + c]) * (1.f / D_);
            *(f32x2*)(p.ws + OFF_ST1 + (size_t)(mt * 32 + c) * 8) = f32x2{mean, rsqrtf(var + 1e-5f)};
        }
        return;
    }
    b -= NB_X;
    {
        mod_slice(p, 896 + 16 * b, 2240 + 16 * b, sm);
        if (t < 32) ((float*)(p.ws + OFF_GAGB))[(t >> 4) * 448 + 16 * b + (t & 15)] = sm[t];
    }
}

__global__ __launch_bounds__(256, 1) void k_qkv(Params p) {
    __shared__ __attribute__((aligned(16))) char smem[90112];
    char* sB = smem;
    float* sGB = (float*)(smem + 84 * 1024);
    float* sST = sGB + 192;
    const int b = blockIdx.x, g = b & 7, idx = b >> 3;
    const int pnl = 2 * g + (idx >> 4), head = idx & 15;
    const int t = threadIdx.x, lane = t & 63, w = t >> 6, c = lane & 31, h = lane >> 5;
    const int mt = pnl * 4 + w;
    {
        const char* src = p.ws + OFF_WQKV + (size_t)head * 3 * KS_D * 1024;
#pragma unroll
        for (int i = 0; i < 21; i++) {
            const int f = w * 21 + i;
            __builtin_amdgcn_global_load_lds((const void*)(src + (size_t)f * 1024 + lane * 16), (__attribute__((address_space(3))) void*)(sB + f * 1024), 16, 0, 0);
        }
    }
    u32x4 a[KS_D];
#pragma unroll
    for (int S = 0; S < KS_D; S++) a[S] = *(const u32x4*)(p.ws + OFF_XF + ((size_t)(mt * KS_D + S) * 64 + lane) * 16);
    if (t < 192) {
        const int col = t % 96, which = t / 96;
        float s = 0.f;
#pragma unroll
        for (int S = 0; S < KS_D; S++) s += ((const float*)(p.ws + OFF_GB1))[((size_t)S * 1536 + head * 96 + col) * 2 + which];
        sGB[which * 96 + col] = s;
    }
    sST[t] = ((const float*)(p.ws + OFF_ST1))[pnl * 256 + t];
    __syncthreads();
    f32x16 aq = {0}, ak = {0}, av = {0};
#pragma unroll
    for (int S = 0; S < KS_D; S++) {
        const u32x4 bq = *(const u32x4*)(sB + (0 * KS_D + S) * 1024 + lane * 16);
        const u32x4 bk = *(const u32x4*)(sB + (1 * KS_D + S) * 1024 + lane * 16);
        const u32x4 bv = *(const u32x4*)(sB + (2 * KS_D + S) * 1024 + lane * 16);
        aq = MFMA(bq, a[S], aq);
        ak = MFMA(bk, a[S], ak);
        av = MFMA(a[S], bv, av);
    }
    {
        const float mu = sST[(w * 32 + c) * 2], r = sST[(w * 32 + c) * 2 + 1];
        const float qs = 0.18898223650461362f * 1.4426950408889634f;
        float q[16], k[16];
#pragma unroll
        for (int a4 = 0; a4 < 4; a4++) {
            const f32x4 Gq = *(const f32x4*)(sGB + 0 + 8 * a4 + 4 * h), Bq = *(const f32x4*)(sGB + 96 + 0 + 8 * a4 + 4 * h);
            const f32x4 Gk = *(const f32x4*)(sGB + 32 + 8 * a4 + 4 * h), Bk = *(const f32x4*)(sGB + 96 + 32 + 8 * a4 + 4 * h);
#pragma unroll
            for (int bb = 0; bb < 4; bb++) {
                q[4 * a4 + bb] = (r * (aq[4 * a4 + bb] - mu * Gq[bb]) + Bq[bb]) * qs;
                k[4 * a4 + bb] = r * (ak[4 * a4 + bb] - mu * Gk[bb]) + Bk[bb];
            }
        }
#pragma unroll
        for (int s = 0; s < 2; s++) {
            u32x4 oq, ok;
#pragma unroll
            for (int j = 0; j < 4; j++) { oq[j] = pk2(q[8 * s + 2 * j], q[8 * s + 2 * j + 1]); ok[j] = pk2(k[8 * s + 2 * j], k[8 * s + 2 * j + 1]); }
            *(u32x4*)(p.ws + OFF_Q + ((size_t)((head * 64 + mt) * 2 + s) * 64 + lane) * 16) = oq;
            *(u32x4*)(p.ws + OFF_K + ((size_t)((head * 64 + mt) * 2 + s) * 64 + lane) * 16) = ok;
        }
    }
    {
        const float G = sGB[64 + c], B = sGB[96 + 64 + c];
        float v[16];
#pragma unroll
        for (int a4 = 0; a4 < 4; a4++) {
            const f32x4 s01 = *(const f32x4*)(sST + (w * 32 + 8 * a4 + 4 * h) * 2);
            const f32x4 s23 = *(const f32x4*)(sST + (w * 32 + 8 * a4 + 4 * h + 2) * 2);
            v[4 * a4 + 0] = s01[1] * (av[4 * a4 + 0] - s01[0] * G) + B;
            v[4 * a4 + 1] = s01[3] * (av[4 * a4 + 1] - s01[2] * G) + B;
            v[4 * a4 + 2] = s23[1] * (av[4 * a4 + 2] - s23[0] * G) + B;
            v[4 * a4 + 3] = s23[3] * (av[4 * a4 + 3] - s23[2] * G) + B;
        }
        if (c == DH) {
#pragma unroll
            for (int i = 0; i < 16; i++) v[i] = 1.f;
        }
#pragma unroll
        for (int s = 0; s < 2; s++) {
            u32x4 ov;
#pragma unroll
            for (int j = 0; j < 4; j++) ov[j] = pk2(v[8 * s + 2 * j], v[8 * s + 2 * j + 1]);
            *(u32x4*)(p.ws + OFF_V + ((size_t)((head * 64 + mt) * 2 + s) * 64 + lane) * 16) = ov;
        }
    }
}

__global__ __launch_bounds__(256, 1) void k_attn(Params p) {
    const int b = blockIdx.x, g = b & 7, idx = b >> 3;
    const int pnl = 2 * g + (idx >> 4), head = idx & 15;
    const int t = threadIdx.x, lane = t & 63, w = t >> 6, c = lane & 31, h = lane >> 5;
    const int T = pnl * 4 + w;
    const char* Qp = p.ws + OFF_Q + (size_t)head * 64 * 2048 + lane * 16;
    const char* Kp = p.ws + OFF_K + (size_t)head * 64 * 2048 + lane * 16;
    const char* Vp = p.ws + OFF_V + (size_t)head * 64 * 2048 + lane * 16;
    const u32x4 q0 = *(const u32x4*)(Qp + (size_t)T * 2048), q1 = *(const u32x4*)(Qp + (size_t)T * 2048 + 1024);
    u32x4 kf[9][2], vf[9][2];
#pragma unroll
    for (int i = 0; i < 9; i++) {
        int kt = T - 8 + i; kt = kt < 0 ? 0 : kt;
        kf[i][0] = *(const u32x4*)(Kp + (size_t)kt * 2048); kf[i][1] = *(const u32x4*)(Kp + (size_t)kt * 2048 + 1024);
    }
#pragma unroll
    for (int i = 0; i < 9; i++) {
        int kt = T - 8 + i; kt = kt < 0 ? 0 : kt;
        vf[i][0] = *(const u32x4*)(Vp + (size_t)kt * 2048); vf[i][1] = *(const u32x4*)(Vp + (size_t)kt * 2048 + 1024);
    }
    f32x16 s[9];
    const float NEG = -1e30f;
#pragma unroll
    for (int i = 0; i < 9; i++) {
        f32x16 z = {0};
        z = MFMA(kf[i][0], q0, z);
        z = MFMA(kf[i][1], q1, z);
        const bool tile_ok = (T - 8 + i) >= 0;
#pragma unroll
        for (int r = 0; r < 16; r++) {
            const int key = (r & 3) + 8 * (r >> 2) + 4 * h;
            bool ok = tile_ok;
            if (i == 0) ok = ok && (key > c);
            if (i == 8) ok = key <= c;
            z[r] = ok ? z[r] : NEG;
        }
        s[i] = z;
    }
    float m = NEG;
#pragma unroll
    for (int i = 0; i < 9; i++)
#pragma unroll
        for (int r = 0; r < 16; r++) m = fmaxf(m, s[i][r]);
    m = fmaxf(m, __shfl_xor(m, 32));
    f32x16 o = {0};
#pragma unroll
    for (int i = 0; i < 9; i++) {
        u32x4 p0, p1;
#pragma unroll
        for (int j = 0; j < 4; j++) {
            p0[j] = pk2(__builtin_amdgcn_exp2f(s[i][2 * j] - m), __builtin_amdgcn_exp2f(s[i][2 * j + 1] - m));
            p1[j] = pk2(__builtin_amdgcn_exp2f(s[i][8 + 2 * j] - m), __builtin_amdgcn_exp2f(s[i][8 + 2 * j + 1] - m));
        }
        o = MFMA(vf[i][0], p0, o);
        o = MFMA(vf[i][1], p1, o);
    }
    const float l = __shfl(o[12], c + 32);
    const float inv = 1.f / l;
#pragma unroll
    for (int sx = 0; sx < 2; sx++) {
        u32x4 ov;
#pragma unroll
        for (int j = 0; j < 4; j++) ov[j] = pk2(o[8 * sx + 2 * j] * inv, o[8 * sx + 2 * j + 1] * inv);
        *(u32x4*)(p.ws + OFF_O + ((size_t)(T * KS_O + head * 2 + sx) * 64 + lane) * 16) = ov;
    }
}

template <int KS>
DI f32x16 gemm_ksplit(const char* Ab, const char* Bb, char* smem, int lane, int w) {
    constexpr int KW = KS / 4;
    f32x16 acc[4];
#pragma unroll
    for (int u = 0; u < 4; u++) acc[u] = f32x16{0};
    const char* A0 = Ab + (size_t)(w * KW) * 1024 + lane * 16;
    const char* B0 = Bb + (size_t)(w * KW) * 1024 + lane * 16;
#pragma unroll
    for (int S = 0; S < KW; S++) {
        const u32x4 a0 = *(const u32x4*)(A0 + S * 1024), a1 = *(const u32x4*)(A0 + (KS + S) * 1024);
        const u32x4 b0 = *(const u32x4*)(B0 + S * 1024), b1 = *(const u32x4*)(B0 + (KS + S) * 1024);
        acc[0] = MFMA(b0, a0, acc[0]);
        acc[1] = MFMA(b1, a0, acc[1]);
        acc[2] = MFMA(b0, a1, acc[2]);
        acc[3] = MFMA(b1, a1, acc[3]);
    }
#pragma unroll
    for (int u = 0; u < 4; u++) {
        if (u != w) {
#pragma unroll
            for (int q = 0; q < 4; q++)
                *(f32x4*)(smem + (((u * 4 + w) * 4 + q) * 64 + lane) * 16) = f32x4{acc[u][4 * q], acc[u][4 * q + 1], acc[u][4 * q + 2], acc[u][4 * q + 3]};
        }
    }
    __syncthreads();
    f32x16 r = (w == 0) ? acc[0] : (w == 1) ? acc[1] : (w == 2) ? acc[2] : acc[3];
#pragma unroll
    for (int src = 0; src < 4; src++) {
        if (src != w) {
#pragma unroll
            for (int q = 0; q < 4; q++) {
                const f32x4 v = *(const f32x4*)(smem + (((w * 4 + src) * 4 + q) * 64 + lane) * 16);
                r[4 * q] += v[0]; r[4 * q + 1] += v[1]; r[4 * q + 2] += v[2]; r[4 * q + 3] += v[3];
            }
        }
    }
    return r;
}

__global__ __launch_bounds__(256, 1) void k_wo(Params p) {
    __shared__ __attribute__((aligned(16))) char smem[65536];
    const int b = blockIdx.x, g = b & 7, idx = b >> 3;
    if (idx >= 28) return;
    const int mt2 = 4 * g + idx / 7, ng = idx % 7;
    const int t = threadIdx.x, lane = t & 63, w = __builtin_amdgcn_readfirstlane(t >> 6), c = lane & 31, h = lane >> 5;
    f32x16 r = gemm_ksplit<KS_O>(p.ws + OFF_O + (size_t)(2 * mt2) * KS_O * 1024, p.ws + OFF_WO + (size_t)(2 * ng) * KS_O * 1024, smem, lane, w);
    const int mt = 2 * mt2 + (w >> 1), nt = 2 * ng + (w & 1);
    const float* ga = (const float*)(p.ws + OFF_GAGB);
    float x1[16];
    float sum = 0.f;
#pragma unroll
    for (int s = 0; s < 2; s++) {
        const u32x4 xv = *(const u32x4*)(p.ws + OFF_XF + ((size_t)(mt * KS_D + 2 * nt + s) * 64 + lane) * 16);
        const f32x4 g0 = *(const f32x4*)(ga + nt * 32 + 16 * s + 4 * h), g1 = *(const f32x4*)(ga + nt * 32 + 16 * s + 8 + 4 * h);
        u32x4 o;
        o[0] = pk2(bflo(xv[0]) + g0[0] * r[8 * s + 0], bfhi(xv[0]) + g0[1] * r[8 * s + 1]);
        o[1] = pk2(bflo(xv[1]) + g0[2] * r[8 * s + 2], bfhi(xv[1]) + g0[3] * r[8 * s + 3]);
        o[2] = pk2(bflo(xv[2]) + g1[0] * r[8 * s + 4], bfhi(xv[2]) + g1[1] * r[8 * s + 5]);
        o[3] = pk2(bflo(xv[3]) + g1[2] * r[8 * s + 6], bfhi(xv[3]) + g1[3] * r[8 * s + 7]);
        *(u32x4*)(p.ws + OFF_X1 + ((size_t)(mt * KS_D + 2 * nt + s) * 64 + lane) * 16) = o;
#pragma unroll
        for (int j = 0; j < 4; j++) { x1[8 * s + 2 * j] = bflo(o[j]); x1[8 * s + 2 * j + 1] = bfhi(o[j]); sum += x1[8 * s + 2 * j] + x1[8 * s + 2 * j + 1]; }
    }
    sum += __shfl_xor(sum, 32);
    const float mean = sum * (1.f / 32.f);
    float m2 = 0.f;
#pragma unroll
    for (int i = 0; i < 16; i++) { const float d = x1[i] - mean; m2 += d * d; }
    m2 += __shfl_xor(m2, 32);
    if (h == 0) *(f32x2*)(p.ws + OFF_ST2 + ((size_t)(mt * 14 + nt) * 32 + c) * 8) = f32x2{mean, m2};
}

__global__ __launch_bounds__(256, 1) void k_w1(Params p) {
    __shared__ __attribute__((aligned(16))) char smem[116736];
    char* sB = smem;
    float* sGB = (float*)(smem + 112 * 1024);
    const int b = blockIdx.x, g = b & 7, idx = b >> 3;
    if (idx >= 28) return;
    const int pnl = 2 * g + idx / 14, cg = idx % 14;
    const int t = threadIdx.x, lane = t & 63, w = t >> 6, c = lane & 31, h = lane >> 5;
    const int mt = pnl * 4 + w;
    {
        const char* src = p.ws + OFF_W1 + (size_t)cg * 4 * KS_D * 1024;
#pragma unroll
        for (int i = 0; i < 28; i++) {
            const int f = w * 28 + i;
            __builtin_amdgcn_global_load_lds((const void*)(src + (size_t)f * 1024 + lane * 16), (__attribute__((address_space(3))) void*)(sB + f * 1024), 16, 0, 0);
        }
    }
    u32x4 a[KS_D];
#pragma unroll
    for (int S = 0; S < KS_D; S++) a[S] = *(const u32x4*)(p.ws + OFF_X1 + ((size_t)(mt * KS_D + S) * 64 + lane) * 16);
    {
        const int col = t & 127, which = t >> 7;
        float s = 0.f;
#pragma unroll
        for (int S = 0; S < KS_D; S++) s += ((const float*)(p.ws + OFF_GB2))[((size_t)S * 1792 + cg * 128 + col) * 2 + which];
        sGB[which * 128 + col] = s;
    }
    float mu, rs;
    {
        f32x2 st[14];
#pragma unroll
        for (int i = 0; i < 14; i++) st[i] = *(const f32x2*)(p.ws + OFF_ST2 + ((size_t)(mt * 14 + i) * 32 + c) * 8);
        float s = 0.f;
#pragma unroll
        for (int i = 0; i < 14; i++) s += st[i][0];
        mu = s * (1.f / 14.f);
        float m2 = 0.f;
#pragma unroll
        for (int i = 0; i < 14; i++) { const float d = st[i][0] - mu; m2 += st[i][1] + 32.f * d * d; }
        rs = rsqrtf(m2 * (1.f / D_) + 1e-5f);
    }
    __syncthreads();
    f32x16 acc[4];
#pragma unroll
    for (int n = 0; n < 4; n++) acc[n] = f32x16{0};
#pragma unroll
    for (int S = 0; S < KS_D; S++) {
#pragma unroll
        for (int n = 0; n < 4; n++) {
            const u32x4 bw = *(const u32x4*)(sB + (n * KS_D + S) * 1024 + lane * 16);
            acc[n] = MFMA(bw, a[S], acc[n]);
        }
    }
#pragma unroll
    for (int n = 0; n < 4; n++) {
        float v[16];
#pragma unroll
        for (int a4 = 0; a4 < 4; a4++) {
            const f32x4 G = *(const f32x4*)(sGB + n * 32 + 8 * a4 + 4 * h), B = *(const f32x4*)(sGB + 128 + n * 32 + 8 * a4 + 4 * h);
#pragma unroll
            for (int bb = 0; bb < 4; bb++) v[4 * a4 + bb] = fmaxf(rs * (acc[n][4 * a4 + bb] - mu * G[bb]) + B[bb], 0.f);
        }
#pragma unroll
        for (int s = 0; s < 2; s++) {
            u32x4 o;
#pragma unroll
            for (int j = 0; j < 4; j++) o[j] = pk2(v[8 * s + 2 * j], v[8 * s + 2 * j + 1]);
            *(u32x4*)(p.ws + OFF_H1 + ((size_t)(mt * KS_F + (cg * 4 + n) * 2 + s) * 64 + lane) * 16) = o;
        }
    }
}

__global__ __launch_bounds__(256, 1) void k_w2(Params p) {
    __shared__ __attribute__((aligned(16))) char smem[65536];
    const int b = blockIdx.x, g = b & 7, idx = b >> 3;
    if (idx >= 28) return;
    const int mt2 = 4 * g + idx / 7, ng = idx % 7;
    const int t = threadIdx.x, lane = t & 63, w = __builtin_amdgcn_readfirstlane(t >> 6), c = lane & 31, h = lane >> 5;
    f32x16 r = gemm_ksplit<KS_F>(p.ws + OFF_H1 + (size_t)(2 * mt2) * KS_F * 1024, p.ws + OFF_W2 + (size_t)(2 * ng) * KS_F * 1024, smem, lane, w);
    const int mt = 2 * mt2 + (w >> 1), nt = 2 * ng + (w & 1);
    const float* gb = (const float*)(p.ws + OFF_GAGB) + 448;
    u16* orow = p.out + (size_t)(mt * 32 + c) * D_ + nt * 32;
#pragma unroll
    for (int s = 0; s < 2; s++) {
        const u32x4 xv = *(const u32x4*)(p.ws + OFF_X1 + ((size_t)(mt * KS_D + 2 * nt + s) * 64 + lane) * 16);
        const f32x4 g0 = *(const f32x4*)(gb + nt * 32 + 16 * s + 4 * h), g1 = *(const f32x4*)(gb + nt * 32 + 16 * s + 8 + 4 * h);
        u32x2 o0, o1;
        o0[0] = pk2(bflo(xv[0]) + g0[0] * r[8 * s + 0], bfhi(xv[0]) + g0[1] * r[8 * s + 1]);
        o0[1] = pk2(bflo(xv[1]) + g0[2] * r[8 * s + 2], bfhi(xv[1]) + g0[3] * r[8 * s + 3]);
        o1[0] = pk2(bflo(xv[2]) + g1[0] * r[8 * s + 4], bfhi(xv[2]) + g1[1] * r[8 * s + 5]);
        o1[1] = pk2(bflo(xv[3]) + g1[2] * r[8 * s + 6], bfhi(xv[3]) + g1[3] * r[8 * s + 7]);
        *(u32x2*)(orow + 16 * s + 4 * h) = o0;
        *(u32x2*)(orow + 16 * s + 8 + 4 * h) = o1;
    }
}

extern "C" void kernel_launch(void* const* d_in, const int* in_sizes, int n_in,
                              void* d_out, int out_size, void* d_ws, size_t ws_size,
                              hipStream_t stream) {
    Params p;
    p.x = (const u16*)d_in[0]; p.wqkv = (const u16*)d_in[1]; p.wo = (const u16*)d_in[2];
    p.w1 = (const u16*)d_in[4]; p.w2 = (const u16*)d_in[5]; p.c = (const u16*)d_in[6]; p.wmod = (const u16*)d_in[7];
    p.out = (u16*)d_out; p.ws = (char*)d_ws;
    k_prep<<<NB_PREP, 256, 0, stream>>>(p);
    k_qkv<<<256, 256, 0, stream>>>(p);
    k_attn<<<256, 256, 0, stream>>>(p);
    k_wo<<<256, 256, 0, stream>>>(p);
    k_w1<<<256, 256, 0, stream>>>(p);
    k_w2<<<256, 256, 0, stream>>>(p);
}
```

```cpp
#include <hip/hip_runtime.h>
#include <stdint.h>

typedef __attribute__((ext_vector_type(8))) __bf16 bf16x8;
typedef __attribute__((ext_vector_type(2))) __bf16 bf16x2;
typedef __attribute__((ext_vector_type(2))) float f32x2;
typedef __attribute__((ext_vector_type(4))) float f32x4;
typedef __attribute__((ext_vector_type(16))) float f32x16;
typedef __attribute__((ext_vector_type(4))) unsigned u32x4;
typedef __attribute__((ext_vector_type(2))) unsigned u32x2;
typedef unsigned short u16;

#define DI __device__ __forceinline__
#define MFMA(a, b, c) __builtin_amdgcn_mfma_f32_32x32x16_bf16(__builtin_bit_cast(bf16x8, (a)), __builtin_bit_cast(bf16x8, (b)), (c), 0, 0, 0)

constexpr int S_ = 2048, D_ = 448, DFF = 1792, NH = 16, DH = 28;
constexpr int KS_D = 28;
constexpr int KS_O = 32;
constexpr int KS_F = 112;

constexpr size_t OFF_FLAGS = 0;
constexpr size_t OFF_WQKV = 65536;
constexpr size_t OFF_WO = OFF_WQKV + 48 * 28 * 1024;
constexpr size_t OFF_W1 = OFF_WO + 14 * 32 * 1024;
constexpr size_t OFF_W2 = OFF_W1 + 56 * 28 * 1024;
constexpr size_t OFF_XF = OFF_W2 + 14 * 112 * 1024;
constexpr size_t OFF_ST1 = OFF_XF + 64 * 28 * 1024;
constexpr size_t OFF_MODP = OFF_ST1 + 2048 * 8;
constexpr size_t OFF_GB2 = OFF_MODP + 81920;
constexpr size_t OFF_W1S = OFF_GB2 + 4 * 1792 * 8;
constexpr size_t OFF_Q = OFF_W1S + 56 * 28 * 1024;
constexpr size_t OFF_K = OFF_Q + 16 * 64 * 2048;
constexpr size_t OFF_V = OFF_K + 16 * 64 * 2048;
constexpr size_t OFF_O = OFF_V + 16 * 64 * 2048;
constexpr size_t OFF_X1 = OFF_O + 64 * 32 * 1024;
constexpr size_t OFF_ST2 = OFF_X1 + 64 * 28 * 1024;
constexpr size_t OFF_H1 = OFF_ST2 + 2048 * 14 * 8;

DI float bf2f(u16 v) { return __uint_as_float(((unsigned)v) << 16); }
DI float bflo(unsigned v) { return __uint_as_float(v << 16); }
DI float bfhi(unsigned v) { return __uint_as_float(v & 0xffff0000u); }
DI unsigned pk2(float a, float b) {
    f32x2 v = {a, b};
    bf16x2 r = __builtin_convertvector(v, bf16x2);
    return __builtin_bit_cast(unsigned, r);
}
DI float rbf(float a) { return bflo(pk2(a, 0.f)); }
DI int kperm(int h, int j) { return 8 * (j >> 2) + 4 * h + (j & 3); }

struct Params {
    const u16* x; const u16* wqkv; const u16* wo; const u16* w1; const u16* w2; const u16* c; const u16* wmod;
    u16* out; char* ws;
};

#define STAMP(kid, i)
#define DRAIN()
#define SB() __builtin_amdgcn_sched_barrier(0)

constexpr int NB_MOD = 42 * 7, NB_WQ = 84, NB_W1 = 98, NB_WO = 28, NB_W2 = 98, NB_X = 64;
constexpr int NB_PREP = NB_MOD + NB_WQ + NB_W1 + NB_WO + NB_W2 + NB_X;

DI u32x4 pack8(const u16* v) {
    u32x4 o;
#pragma unroll
    for (int j = 0; j < 4; j++) o[j] = (unsigned)v[2 * j] | ((unsigned)v[2 * j + 1] << 16);
    return o;
}

__global__ __launch_bounds__(256) void k_prep(Params p) {
    __shared__ float sm[256];
    int b = blockIdx.x;
    const int t = threadIdx.x, lane = t & 63, w = t >> 6, c = lane & 31, h = lane >> 5;
    STAMP(0, 0);
    if (b < NB_MOD) {
        const int cgp = b % 42, kq = b / 42;
        const int pc = t & 7, rs = t >> 3;
        u32x4 wv[2]; u16 cv[2];
#pragma unroll
        for (int i = 0; i < 2; i++) {
            const int row = 64 * kq + rs + 32 * i;
            wv[i] = *(const u32x4*)(p.wmod + (size_t)row * 2688 + cgp * 64 + pc * 8);
            cv[i] = p.c[row];
        }
        float acc[8];
#pragma unroll
        for (int j = 0; j < 8; j++) acc[j] = 0.f;
#pragma unroll
        for (int i = 0; i < 2; i++) {
            const float cf = bf2f(cv[i]);
            const float s = rbf(cf / (1.f + __expf(-cf)));
#pragma unroll
            for (int j = 0; j < 4; j++) { acc[2 * j] += s * bflo(wv[i][j]); acc[2 * j + 1] += s * bfhi(wv[i][j]); }
        }
#pragma unroll
        for (int j = 0; j < 8; j++) { acc[j] += __shfl_xor(acc[j], 8); acc[j] += __shfl_xor(acc[j], 16); acc[j] += __shfl_xor(acc[j], 32); }
        if (lane < 8) {
#pragma unroll
            for (int j = 0; j < 8; j++) sm[w * 64 + pc * 8 + j] = acc[j];
        }
        __syncthreads();
        if (t < 64) ((float*)(p.ws + OFF_MODP))[kq * 2688 + cgp * 64 + t] = sm[t] + sm[64 + t] + sm[128 + t] + sm[192 + t];
        DRAIN(); STAMP(0, 1);
        return;
    }
    b -= NB_MOD;
    if (b < NB_X) {
        const int mt = b;
        const u16* row = p.x + (size_t)(mt * 32 + c) * D_;
        u32x4 v[7];
        float s = 0.f;
#pragma unroll
        for (int i = 0; i < 7; i++) {
            const int S = w * 7 + i;
            u32x2 a = *(const u32x2*)(row + 16 * S + 4 * h);
            u32x2 bb = *(const u32x2*)(row + 16 * S + 8 + 4 * h);
            v[i] = u32x4{a[0], a[1], bb[0], bb[1]};
        }
#pragma unroll
        for (int i = 0; i < 7; i++)
#pragma unroll
            for (int j = 0; j < 4; j++) s += bflo(v[i][j]) + bfhi(v[i][j]);
        s += __shfl_xor(s, 32);
        if (h == 0) sm[w * 32 + c] = s;
        __syncthreads();
        const float mean = (sm[c] + sm[32 + c] + sm[64 + c] + sm[96 + c]) * (1.f / D_);
        float q = 0.f;
#pragma unroll
        for (int i = 0; i < 7; i++)
#pragma unroll
            for (int j = 0; j < 4; j++) { float a = bflo(v[i][j]) - mean, bb = bfhi(v[i][j]) - mean; q += a * a + bb * bb; }
        q += __shfl_xor(q, 32);
        if (h == 0) sm[128 + w * 32 + c] = q;
        __syncthreads();
        const float var = (sm[128 + c] + sm[160 + c] + sm[192 + c] + sm[224 + c]) * (1.f / D_);
        const float r = rsqrtf(var + 1e-5f);
#pragma unroll
        for (int i = 0; i < 7; i++) {
            u32x4 o;
#pragma unroll
            for (int j = 0; j < 4; j++) o[j] = pk2((bflo(v[i][j]) - mean) * r, (bfhi(v[i][j]) - mean) * r);
            *(u32x4*)(p.ws + OFF_XF + ((size_t)(mt * KS_D + w * 7 + i) * 64 + lane) * 16) = o;
        }
        DRAIN(); STAMP(0, 2);
        return;
    }
    b -= NB_X;
    if (b < NB_WQ) {
        u16 v[4][8];
#pragma unroll
        for (int i = 0; i < 4; i++) {
            const int f = b * 16 + w * 4 + i, S = f / 48, r48 = f % 48;
#pragma unroll
            for (int j = 0; j < 8; j++) v[i][j] = (c < DH) ? p.wqkv[(size_t)(16 * S + kperm(h, j)) * 1344 + r48 * DH + c] : (u16)0;
        }
#pragma unroll
        for (int i = 0; i < 4; i++) {
            const int f = b * 16 + w * 4 + i, S = f / 48, r48 = f % 48, tt = r48 >> 4, head = r48 & 15;
            *(u32x4*)(p.ws + OFF_WQKV + ((size_t)((head * KS_D + S) * 3 + tt) * 64 + lane) * 16) = pack8(v[i]);
        }
        DRAIN(); STAMP(0, 3);
        return;
    }
    b -= NB_WQ;
    if (b < NB_W1) {
        u16 v[4][8];
#pragma unroll
        for (int i = 0; i < 4; i++) {
            const int f = b * 16 + w * 4 + i, S = f / 56, nt = f % 56;
#pragma unroll
            for (int j = 0; j < 8; j++) v[i][j] = p.w1[(size_t)(16 * S + kperm(h, j)) * DFF + nt * 32 + c];
        }
#pragma unroll
        for (int i = 0; i < 4; i++) {
            const int f = b * 16 + w * 4 + i, S = f / 56, nt = f % 56;
            *(u32x4*)(p.ws + OFF_W1 + ((size_t)(nt * KS_D + S) * 64 + lane) * 16) = pack8(v[i]);
        }
        DRAIN(); STAMP(0, 4);
        return;
    }
    b -= NB_W1;
    if (b < NB_WO) {
        u16 v[4][8];
#pragma unroll
        for (int i = 0; i < 4; i++) {
            const int f = b * 16 + w * 4 + i, Sp = f / 14, nt = f % 14, head = Sp >> 1, s = Sp & 1;
#pragma unroll
            for (int j = 0; j < 8; j++) {
                const int d = 16 * s + kperm(h, j);
                v[i][j] = (d < DH) ? p.wo[(size_t)(head * DH + d) * D_ + nt * 32 + c] : (u16)0;
            }
        }
#pragma unroll
        for (int i = 0; i < 4; i++) {
            const int f = b * 16 + w * 4 + i, Sp = f / 14, nt = f % 14;
            *(u32x4*)(p.ws + OFF_WO + ((size_t)(nt * KS_O + Sp) * 64 + lane) * 16) = pack8(v[i]);
        }
        DRAIN(); STAMP(0, 5);
        return;
    }
    b -= NB_WO;
    {
        u16 v[4][8];
#pragma unroll
        for (int i = 0; i < 4; i++) {
            const int f = b * 16 + w * 4 + i, S = f / 14, nt = f % 14;
#pragma unroll
            for (int j = 0; j < 8; j++) v[i][j] = p.w2[(size_t)(16 * S + kperm(h, j)) * D_ + nt * 32 + c];
        }
#pragma unroll
        for (int i = 0; i < 4; i++) {
            const int f = b * 16 + w * 4 + i, S = f / 14, nt = f % 14;
            *(u32x4*)(p.ws + OFF_W2 + ((size_t)(nt * KS_F + S) * 64 + lane) * 16) = pack8(v[i]);
        }
        DRAIN(); STAMP(0, 6);
    }
}

DI float mod_val(const Params& p, int col) {
    const float* mp = (const float*)(p.ws + OFF_MODP);
    float s = 0.f;
#pragma unroll
    for (int i = 0; i < 7; i++) s += mp[i * 2688 + col];
    return rbf(s);
}

DI void w1_scale_task(const Params& p, int task, int lane) {
    const int nt = task >> 2, kq = task & 3, c = lane & 31, h = lane >> 5;
    u32x4 wv[7];
#pragma unroll
    for (int i = 0; i < 7; i++) wv[i] = *(const u32x4*)(p.ws + OFF_W1 + ((size_t)(nt * KS_D + kq * 7 + i) * 64 + lane) * 16);
    const int k0 = kq * 112 + lane, k1 = kq * 112 + 64 + (lane < 48 ? lane : 0);
    const float g0 = rbf(1.f + mod_val(p, 1792 + k0)), s0 = mod_val(p, 1344 + k0);
    const float g1 = rbf(1.f + mod_val(p, 1792 + k1)), s1 = mod_val(p, 1344 + k1);
    float G = 0.f, B = 0.f;
#pragma unroll
    for (int i = 0; i < 7; i++) {
        u32x4 o;
#pragma unroll
        for (int j = 0; j < 4; j++) {
            const int ka = 16 * i + kperm(h, 2 * j), kb = ka + 1;
            const float ga = (i < 4) ? __shfl(g0, ka) : __shfl(g1, ka - 64), gb = (i < 4) ? __shfl(g0, kb) : __shfl(g1, kb - 64);
            const float sa = (i < 4) ? __shfl(s0, ka) : __shfl(s1, ka - 64), sb = (i < 4) ? __shfl(s0, kb) : __shfl(s1, kb - 64);
            const float wa = bflo(wv[i][j]), wb = bfhi(wv[i][j]);
            o[j] = pk2(wa * ga, wb * gb);
            G += bflo(o[j]) + bfhi(o[j]);
            B += wa * sa + wb * sb;
        }
        *(u32x4*)(p.ws + OFF_W1S + ((size_t)(nt * KS_D + kq * 7 + i) * 64 + lane) * 16) = o;
    }
    G += __shfl_xor(G, 32); B += __shfl_xor(B, 32);
    if (h == 0) *(f32x2*)(p.ws + OFF_GB2 + ((size_t)kq * 1792 + nt * 32 + c) * 8) = f32x2{G, B};
}

__global__ __launch_bounds__(256, 1) void k_qkv(Params p) {
    __shared__ __attribute__((aligned(16))) char smem[88 * 1024];
    char* sB = smem;
    float* sGS = (float*)(smem + 84 * 1024);
    const int b = blockIdx.x, g = b & 7, idx = b >> 3;
    const int pnl = 2 * g + (idx >> 4), head = idx & 15;
    const int t = threadIdx.x, lane = t & 63, w = __builtin_amdgcn_readfirstlane(t >> 6), c = lane & 31, h = lane >> 5;
    const int mt = pnl * 4 + w;
    STAMP(1, 0);
    float pg[2], ps[2];
#pragma unroll
    for (int i = 0; i < 2; i++) {
        const int k = t + 256 * i;
        if (k < D_) { pg[i] = rbf(1.f + mod_val(p, 448 + k)); ps[i] = mod_val(p, k); }
    }
    SB();
    u32x4 a[KS_D], bs[21];
    const char* srcB = p.ws + OFF_WQKV + (size_t)head * 3 * KS_D * 1024 + lane * 16;
    const char* srcA = p.ws + OFF_XF + (size_t)mt * KS_D * 1024 + lane * 16;
#pragma unroll
    for (int sg = 0; sg < 7; sg++) {
#pragma unroll
        for (int i = 0; i < 3; i++) bs[sg * 3 + i] = *(const u32x4*)(srcB + (size_t)(sg * 12 + w * 3 + i) * 1024);
#pragma unroll
        for (int i = 0; i < 4; i++) a[sg * 4 + i] = *(const u32x4*)(srcA + (size_t)(sg * 4 + i) * 1024);
        SB();
    }
    if (b < 224 && false) w1_scale_task(p, b, lane);
#pragma unroll
    for (int i = 0; i < 2; i++) {
        const int k = t + 256 * i;
        if (k < D_) {
            const int S = k >> 4, kk = k & 15, j = (kk & 3) + 4 * (kk >> 3), hh = (kk >> 2) & 1;
            sGS[(S * 2 + hh) * 16 + j] = pg[i];
            sGS[(S * 2 + hh) * 16 + 8 + j] = ps[i];
        }
    }
#pragma unroll
    for (int i = 0; i < 3; i++) *(u32x4*)(sB + (w * 3 + i) * 1024 + lane * 16) = bs[i];
    __syncthreads();
    STAMP(1, 1);
    f32x16 aq = {0}, ak = {0}, av = {0};
#pragma unroll
    for (int sg = 0; sg < 7; sg++) {
#pragma unroll
        for (int i = 0; i < 4; i++) {
            const int S = sg * 4 + i;
            const f32x4 g0 = *(const f32x4*)(sGS + (S * 2 + h) * 16), g1 = *(const f32x4*)(sGS + (S * 2 + h) * 16 + 4);
            const f32x4 s0 = *(const f32x4*)(sGS + (S * 2 + h) * 16 + 8), s1 = *(const f32x4*)(sGS + (S * 2 + h) * 16 + 12);
            const u32x4 bq = *(const u32x4*)(sB + (S * 3 + 0) * 1024 + lane * 16);
            const u32x4 bk = *(const u32x4*)(sB + (S * 3 + 1) * 1024 + lane * 16);
            const u32x4 bv = *(const u32x4*)(sB + (S * 3 + 2) * 1024 + lane * 16);
            u32x4 hf;
            hf[0] = pk2(bflo(a[S][0]) * g0[0] + s0[0], bfhi(a[S][0]) * g0[1] + s0[1]);
            hf[1] = pk2(bflo(a[S][1]) * g0[2] + s0[2], bfhi(a[S][1]) * g0[3] + s0[3]);
            hf[2] = pk2(bflo(a[S][2]) * g1[0] + s1[0], bfhi(a[S][2]) * g1[1] + s1[1]);
            hf[3] = pk2(bflo(a[S][3]) * g1[2] + s1[2], bfhi(a[S][3]) * g1[3] + s1[3]);
            ak = MFMA(bk, hf, ak);
            av = MFMA(hf, bv, av);
            aq = MFMA(bq, hf, aq);
        }
        if (sg < 6) {
#pragma unroll
            for (int i = 0; i < 3; i++) *(u32x4*)(sB + ((sg + 1) * 12 + w * 3 + i) * 1024 + lane * 16) = bs[(sg + 1) * 3 + i];
            __syncthreads();
        }
    }
    STAMP(1, 2);
    {
        const float qs = 0.18898223650461362f * 1.4426950408889634f;
        if (c == DH) {
#pragma unroll
            for (int i = 0; i < 16; i++) av[i] = 1.f;
        }
#pragma unroll
        for (int s = 0; s < 2; s++) {
            u32x4 oq, ok, ov;
#pragma unroll
            for (int j = 0; j < 4; j++) {
                oq[j] = pk2(aq[8 * s + 2 * j] * qs, aq[8 * s + 2 * j + 1] * qs);
                ok[j] = pk2(ak[8 * s + 2 * j], ak[8 * s + 2 * j + 1]);
                ov[j] = pk2(av[8 * s + 2 * j], av[8 * s + 2 * j + 1]);
            }
            *(u32x4*)(p.ws + OFF_K + ((size_t)((head * 64 + mt) * 2 + s) * 64 + lane) * 16) = ok;
            *(u32x4*)(p.ws + OFF_V + ((size_t)((head * 64 + mt) * 2 + s) * 64 + lane) * 16) = ov;
            *(u32x4*)(p.ws + OFF_Q + ((size_t)((head * 64 + mt) * 2 + s) * 64 + lane) * 16) = oq;
        }
    }
    STAMP(1, 3);
    if (b < 224 && w == 0) w1_scale_task(p, b, lane);
    DRAIN(); STAMP(1, 4);
}

__global__ __launch_bounds__(256, 1) void k_attn(Params p) {
    const int b = blockIdx.x, g = b & 7, idx = b >> 3;
    const int pnl = 2 * g + (idx >> 4), head = idx & 15;
    const int t = threadIdx.x, lane = t & 63, w = __builtin_amdgcn_readfirstlane(t >> 6), c = lane & 31, h = lane >> 5;
    const int T = pnl * 4 + w;
    STAMP(2, 0);
    const char* Qp = p.ws + OFF_Q + (size_t)head * 64 * 2048 + lane * 16;
    const char* Kp = p.ws + OFF_K + (size_t)head * 64 * 2048 + lane * 16;
    const char* Vp = p.ws + OFF_V + (size_t)head * 64 * 2048 + lane * 16;
    const u32x4 q0 = *(const u32x4*)(Qp + (size_t)T * 2048), q1 = *(const u32x4*)(Qp + (size_t)T * 2048 + 1024);
    u32x4 kf[9][2], vf[9][2];
#pragma unroll
    for (int i = 0; i < 9; i++) {
        int kt = T - 8 + i; kt = kt < 0 ? 0 : kt;
        kf[i][0] = *(const u32x4*)(Kp + (size_t)kt * 2048); kf[i][1] = *(const u32x4*)(Kp + (size_t)kt * 2048 + 1024);
    }
#pragma unroll
    for (int i = 0; i < 9; i++) {
        int kt = T - 8 + i; kt = kt < 0 ? 0 : kt;
        vf[i][0] = *(const u32x4*)(Vp + (size_t)kt * 2048); vf[i][1] = *(const u32x4*)(Vp + (size_t)kt * 2048 + 1024);
    }
    SB();
    f32x16 s[9];
    const float NEG = -1e30f;
    float m = NEG;
#pragma unroll
    for (int i = 0; i < 9; i++) {
        f32x16 z = {0};
        z = MFMA(kf[i][0], q0, z);
        z = MFMA(kf[i][1], q1, z);
        if (i == 0 || i == 8) {
#pragma unroll
            for (int r = 0; r < 16; r++) {
                const int key = (r & 3) + 8 * (r >> 2) + 4 * h;
                const bool ok = (i == 0) ? (key > c) : (key <= c);
                z[r] = ok ? z[r] : NEG;
            }
        }
        if (T - 8 + i < 0) {
#pragma unroll
            for (int r = 0; r < 16; r++) z[r] = NEG;
        }
        s[i] = z;
#pragma unroll
        for (int r = 0; r < 16; r += 2) m = fmaxf(fmaxf(m, z[r]), z[r + 1]);
    }
    STAMP(2, 1);
    m = fmaxf(m, __shfl_xor(m, 32));
    f32x16 o = {0};
#pragma unroll
    for (int i = 0; i < 9; i++) {
        u32x4 p0, p1;
#pragma unroll
        for (int j = 0; j < 4; j++) {
            p0[j] = pk2(__builtin_amdgcn_exp2f(s[i][2 * j] - m), __builtin_amdgcn_exp2f(s[i][2 * j + 1] - m));
            p1[j] = pk2(__builtin_amdgcn_exp2f(s[i][8 + 2 * j] - m), __builtin_amdgcn_exp2f(s[i][8 + 2 * j + 1] - m));
        }
        o = MFMA(vf[i][0], p0, o);
        o = MFMA(vf[i][1], p1, o);
    }
    STAMP(2, 2);
    const float l = __shfl(o[12], c + 32);
    const float inv = 1.f / l;
#pragma unroll
    for (int sx = 0; sx < 2; sx++) {
        u32x4 ov;
#pragma unroll
        for (int j = 0; j < 4; j++) ov[j] = pk2(o[8 * sx + 2 * j] * inv, o[8 * sx + 2 * j + 1] * inv);
        *(u32x4*)(p.ws + OFF_O + ((size_t)(T * KS_O + head * 2 + sx) * 64 + lane) * 16) = ov;
    }
    DRAIN(); STAMP(2, 3);
}

DI f32x16 reduce4(f32x16* acc, char* smem, int lane, int w) {
#pragma unroll
    for (int u = 0; u < 4; u++) {
        if (u != w) {
#pragma unroll
            for (int q = 0; q < 4; q++)
                *(f32x4*)(smem + (((u * 4 + w) * 4 + q) * 64 + lane) * 16) = f32x4{acc[u][4 * q], acc[u][4 * q + 1], acc[u][4 * q + 2], acc[u][4 * q + 3]};
        }
    }
    __syncthreads();
    f32x16 r = (w == 0) ? acc[0] : (w == 1) ? acc[1] : (w == 2) ? acc[2] : acc[3];
#pragma unroll
    for (int src = 0; src < 4; src++) {
        if (src != w) {
#pragma unroll
            for (int q = 0; q < 4; q++) {
                const f32x4 v = *(const f32x4*)(smem + (((w * 4 + src) * 4 + q) * 64 + lane) * 16);
                r[4 * q] += v[0]; r[4 * q + 1] += v[1]; r[4 * q + 2] += v[2]; r[4 * q + 3] += v[3];
            }
        }
    }
    return r;
}

template <int KS, int GS, int PF>
DI void gemm_ksplit(const char* Ab, const char* Bb, int lane, int w, f32x16* acc) {
    constexpr int KW = KS / 4, NG = KW / GS;
#pragma unroll
    for (int u = 0; u < 4; u++) acc[u] = f32x16{0};
    const char* A0 = Ab + (size_t)(w * KW) * 1024 + lane * 16;
    const char* B0 = Bb + (size_t)(w * KW) * 1024 + lane * 16;
    u32x4 a0[KW], a1[KW], b0[KW], b1[KW];
#pragma unroll
    for (int gi = 0; gi < NG + PF; gi++) {
        if (gi < NG) {
#pragma unroll
            for (int i = 0; i < GS; i++) {
                const int S = gi * GS + i;
                b0[S] = *(const u32x4*)(B0 + S * 1024); b1[S] = *(const u32x4*)(B0 + (KS + S) * 1024);
                a0[S] = *(const u32x4*)(A0 + S * 1024); a1[S] = *(const u32x4*)(A0 + (KS + S) * 1024);
            }
        }
        SB();
        if (gi >= PF) {
#pragma unroll
            for (int i = 0; i < GS; i++) {
                const int S = (gi - PF) * GS + i;
                acc[0] = MFMA(b0[S], a0[S], acc[0]);
                acc[1] = MFMA(b1[S], a0[S], acc[1]);
                acc[2] = MFMA(b0[S], a1[S], acc[2]);
                acc[3] = MFMA(b1[S], a1[S], acc[3]);
            }
            SB();
        }
    }
}

__global__ __launch_bounds__(256, 1) void k_wo(Params p) {
    __shared__ __attribute__((aligned(16))) char smem[64 * 1024];
    const int b = blockIdx.x, g = b & 7, idx = b >> 3;
    if (idx >= 28) return;
    const int mt2 = 4 * g + idx / 7, ng = idx % 7;
    const int t = threadIdx.x, lane = t & 63, w = __builtin_amdgcn_readfirstlane(t >> 6), c = lane & 31, h = lane >> 5;
    const int mt = 2 * mt2 + (w >> 1), nt = 2 * ng + (w & 1);
    STAMP(3, 0);
    u32x2 xr[4]; float gav[16];
    {
        const u16* xrow = p.x + (size_t)(mt * 32 + c) * D_ + nt * 32 + 4 * h;
#pragma unroll
        for (int a4 = 0; a4 < 4; a4++) xr[a4] = *(const u32x2*)(xrow + 8 * a4);
        const float gl = mod_val(p, 896 + nt * 32 + c);
#pragma unroll
        for (int i = 0; i < 16; i++) gav[i] = __shfl(gl, (i & 3) + 8 * (i >> 2) + 4 * h);
    }
    SB();
    f32x16 acc[4];
    gemm_ksplit<KS_O, 8, 1>(p.ws + OFF_O + (size_t)(2 * mt2) * KS_O * 1024, p.ws + OFF_WO + (size_t)(2 * ng) * KS_O * 1024, lane, w, acc);
    STAMP(3, 1);
    f32x16 r = reduce4(acc, smem, lane, w);
    STAMP(3, 2);
    float x1[16];
    float sum = 0.f;
#pragma unroll
    for (int s = 0; s < 2; s++) {
        u32x4 o;
#pragma unroll
        for (int j = 0; j < 4; j++) {
            const int i = 8 * s + 2 * j;
            const unsigned xv = xr[i >> 2][(i >> 1) & 1];
            o[j] = pk2(bflo(xv) + gav[i] * r[i], bfhi(xv) + gav[i + 1] * r[i + 1]);
            x1[i] = bflo(o[j]); x1[i + 1] = bfhi(o[j]); sum += x1[i] + x1[i + 1];
        }
        *(u32x4*)(p.ws + OFF_X1 + ((size_t)(mt * KS_D + 2 * nt + s) * 64 + lane) * 16) = o;
    }
    sum += __shfl_xor(sum, 32);
    const float mean = sum * (1.f / 32.f);
    float m2 = 0.f;
#pragma unroll
    for (int i = 0; i < 16; i++) { const float d = x1[i] - mean; m2 += d * d; }
    m2 += __shfl_xor(m2, 32);
    if (h == 0) *(f32x2*)(p.ws + OFF_ST2 + ((size_t)(mt * 14 + nt) * 32 + c) * 8) = f32x2{mean, m2};
    DRAIN(); STAMP(3, 3);
}

__global__ __launch_bounds__(256, 1) void k_w1(Params p) {
    __shared__ __attribute__((aligned(16))) char smem[114 * 1024];
    char* sB = smem;
    float* sGB = (float*)(smem + 112 * 1024);
    const int b = blockIdx.x, g = b & 7, idx = b >> 3;
    if (idx >= 28) return;
    const int pnl = 2 * g + idx / 14, cg = idx % 14;
    const int t = threadIdx.x, lane = t & 63, w = __builtin_amdgcn_readfirstlane(t >> 6), c = lane & 31, h = lane >> 5;
    const int mt = pnl * 4 + w;
    STAMP(4, 0);
    float gbv = 0.f;
    f32x2 st[14];
    {
        const int col = t & 127, which = t >> 7;
#pragma unroll
        for (int q = 0; q < 4; q++) gbv += ((const float*)(p.ws + OFF_GB2))[((size_t)q * 1792 + cg * 128 + col) * 2 + which];
#pragma unroll
        for (int i = 0; i < 14; i++) st[i] = *(const f32x2*)(p.ws + OFF_ST2 + ((size_t)(mt * 14 + i) * 32 + c) * 8);
    }
    SB();
    u32x4 a[KS_D], bs[KS_D];
    const char* srcB = p.ws + OFF_W1S + (size_t)(cg * 4 + w) * KS_D * 1024 + lane * 16;
    const char* srcA = p.ws + OFF_X1 + (size_t)mt * KS_D * 1024 + lane * 16;
#pragma unroll
    for (int sg = 0; sg < 7; sg++) {
#pragma unroll
        for (int i = 0; i < 4; i++) bs[sg * 4 + i] = *(const u32x4*)(srcB + (size_t)(sg * 4 + i) * 1024);
#pragma unroll
        for (int i = 0; i < 4; i++) a[sg * 4 + i] = *(const u32x4*)(srcA + (size_t)(sg * 4 + i) * 1024);
        SB();
    }
    sGB[t] = gbv;
    float mu, rs;
    {
        float s = 0.f;
#pragma unroll
        for (int i = 0; i < 14; i++) s += st[i][0];
        mu = s * (1.f / 14.f);
        float m2 = 0.f;
#pragma unroll
        for (int i = 0; i < 14; i++) { const float d = st[i][0] - mu; m2 += st[i][1] + 32.f * d * d; }
        rs = rsqrtf(m2 * (1.f / D_) + 1e-5f);
    }
#pragma unroll
    for (int i = 0; i < 4; i++) *(u32x4*)(sB + (i * 4 + w) * 1024 + lane * 16) = bs[i];
    __syncthreads();
    STAMP(4, 1);
    f32x16 acc[4];
#pragma unroll
    for (int n = 0; n < 4; n++) acc[n] = f32x16{0};
#pragma unroll
    for (int sg = 0; sg < 7; sg++) {
#pragma unroll
        for (int i = 0; i < 4; i++) {
            const int S = sg * 4 + i;
#pragma unroll
            for (int n = 0; n < 4; n++) {
                const u32x4 bw = *(const u32x4*)(sB + (S * 4 + n) * 1024 + lane * 16);
                acc[n] = MFMA(bw, a[S], acc[n]);
            }
        }
        if (sg < 6) {
#pragma unroll
            for (int i = 0; i < 4; i++) *(u32x4*)(sB + (((sg + 1) * 4 + i) * 4 + w) * 1024 + lane * 16) = bs[(sg + 1) * 4 + i];
            __syncthreads();
        }
    }
    STAMP(4, 2);
#pragma unroll
    for (int n = 0; n < 4; n++) {
        float v[16];
#pragma unroll
        for (int a4 = 0; a4 < 4; a4++) {
            const f32x4 G = *(const f32x4*)(sGB + n * 32 + 8 * a4 + 4 * h), B = *(const f32x4*)(sGB + 128 + n * 32 + 8 * a4 + 4 * h);
#pragma unroll
            for (int bb = 0; bb < 4; bb++) v[4 * a4 + bb] = fmaxf(rs * (acc[n][4 * a4 + bb] - mu * G[bb]) + B[bb], 0.f);
        }
#pragma unroll
        for (int s = 0; s < 2; s++) {
            u32x4 o;
#pragma unroll
            for (int j = 0; j < 4; j++) o[j] = pk2(v[8 * s + 2 * j], v[8 * s + 2 * j + 1]);
            *(u32x4*)(p.ws + OFF_H1 + ((size_t)(mt * KS_F + (cg * 4 + n) * 2 + s) * 64 + lane) * 16) = o;
        }
    }
    DRAIN(); STAMP(4, 3);
}

__global__ __launch_bounds__(256, 1) void k_w2(Params p) {
    __shared__ __attribute__((aligned(16))) char smem[64 * 1024];
    const int b = blockIdx.x, g = b & 7, idx = b >> 3;
    if (idx >= 28) return;
    const int mt2 = 4 * g + idx / 7, ng = idx % 7;
    const int t = threadIdx.x, lane = t & 63, w = __builtin_amdgcn_readfirstlane(t >> 6), c = lane & 31, h = lane >> 5;
    const int mt = 2 * mt2 + (w >> 1), nt = 2 * ng + (w & 1);
    STAMP(5, 0);
    u32x4 xv[2]; float gbv[16];
    {
#pragma unroll
        for (int s = 0; s < 2; s++) xv[s] = *(const u32x4*)(p.ws + OFF_X1 + ((size_t)(mt * KS_D + 2 * nt + s) * 64 + lane) * 16);
        const float gl = mod_val(p, 2240 + nt * 32 + c);
#pragma unroll
        for (int i = 0; i < 16; i++) gbv[i] = __shfl(gl, (i & 3) + 8 * (i >> 2) + 4 * h);
    }
    SB();
    f32x16 acc[4];
    gemm_ksplit<KS_F, 4, 3>(p.ws + OFF_H1 + (size_t)(2 * mt2) * KS_F * 1024, p.ws + OFF_W2 + (size_t)(2 * ng) * KS_F * 1024, lane, w, acc);
    STAMP(5, 1);
    f32x16 r = reduce4(acc, smem, lane, w);
    STAMP(5, 2);
    u16* orow = p.out + (size_t)(mt * 32 + c) * D_ + nt * 32;
#pragma unroll
    for (int s = 0; s < 2; s++) {
        u32x2 o0, o1;
        o0[0] = pk2(bflo(xv[s][0]) + gbv[8 * s + 0] * r[8 * s + 0], bfhi(xv[s][0]) + gbv[8 * s + 1] * r[8 * s + 1]);
        o0[1] = pk2(bflo(xv[s][1]) + gbv[8 * s + 2] * r[8 * s + 2], bfhi(xv[s][1]) + gbv[8 * s + 3] * r[8 * s + 3]);
        o1[0] = pk2(bflo(xv[s][2]) + gbv[8 * s + 4] * r[8 * s + 4], bfhi(xv[s][2]) + gbv[8 * s + 5] * r[8 * s + 5]);
        o1[1] = pk2(bflo(xv[s][3]) + gbv[8 * s + 6] * r[8 * s + 6], bfhi(xv[s][3]) + gbv[8 * s + 7] * r[8 * s + 7]);
        *(u32x2*)(orow + 16 * s + 4 * h) = o0;
        *(u32x2*)(orow + 16 * s + 8 + 4 * h) = o1;
    }
    DRAIN(); STAMP(5, 3);
}

extern "C" void kernel_launch(void* const* d_in, const int* in_sizes, int n_in,
                              void* d_out, int out_size, void* d_ws, size_t ws_size,
                              hipStream_t stream) {
    Params p;
    p.x = (const u16*)d_in[0]; p.wqkv = (const u16*)d_in[1]; p.wo = (const u16*)d_in[2];
    p.w1 = (const u16*)d_in[4]; p.w2 = (const u16*)d_in[5]; p.c = (const u16*)d_in[6]; p.wmod = (const u16*)d_in[7];
    p.out = (u16*)d_out; p.ws = (char*)d_ws;
    k_prep<<<NB_PREP, 256, 0, stream>>>(p);
    k_qkv<<<256, 256, 0, stream>>>(p);
    k_attn<<<256, 256, 0, stream>>>(p);
    k_wo<<<256, 256, 0, stream>>>(p);
    k_w1<<<256, 256, 0, stream>>>(p);
    k_w2<<<256, 256, 0, stream>>>(p);
}
```

```cpp
#include <hip/hip_runtime.h>
#include <stdint.h>

typedef __attribute__((ext_vector_type(8))) __bf16 bf16x8;
typedef __attribute__((ext_vector_type(2))) __bf16 bf16x2;
typedef __attribute__((ext_vector_type(2))) float f32x2;
typedef __attribute__((ext_vector_type(4))) float f32x4;
typedef __attribute__((ext_vector_type(16))) float f32x16;
typedef __attribute__((ext_vector_type(4))) unsigned u32x4;
typedef __attribute__((ext_vector_type(2))) unsigned u32x2;
typedef __attribute__((ext_vector_type(4))) short s16x4;
typedef unsigned short u16;
typedef unsigned long long u64;

#define DI __device__ __forceinline__
#define MFMA(a, b, c) __builtin_amdgcn_mfma_f32_32x32x16_bf16(__builtin_bit_cast(bf16x8, (a)), __builtin_bit_cast(bf16x8, (b)), (c), 0, 0, 0)
#define MFMA16(a, b, c) __builtin_amdgcn_mfma_f32_16x16x32_bf16(__builtin_bit_cast(bf16x8, (a)), __builtin_bit_cast(bf16x8, (b)), (c), 0, 0, 0)
#define SB() __builtin_amdgcn_sched_barrier(0)
#define RLX_AGENT __ATOMIC_RELAXED, __HIP_MEMORY_SCOPE_AGENT
#define LAS __attribute__((address_space(3)))

extern "C" __device__ unsigned long long llvm_dispatch_id() __asm("llvm.amdgcn.dispatch.id");

constexpr int D_ = 448, DFF = 1792, DH = 28;
constexpr int KS_D = 28, KS_O = 32, KS_F = 112;

constexpr unsigned OFF_FLAGS = 0;
constexpr unsigned OFF_WO = 2097152;
constexpr unsigned OFF_W2 = OFF_WO + 14 * 32 * 1024;
constexpr unsigned OFF_MODP = OFF_W2 + 14 * 112 * 1024;
constexpr unsigned OFF_GB2 = OFF_MODP + 49152;
constexpr unsigned OFF_W1S = OFF_GB2 + 14 * 1792 * 8;
constexpr unsigned OFF_KV = OFF_W1S + 56 * 28 * 1024;
constexpr unsigned OFF_O = OFF_KV + 16 * 64 * 4096;
constexpr unsigned OFF_X1 = OFF_O + 64 * 32 * 1024;
constexpr unsigned OFF_ST2 = OFF_X1 + 64 * 28 * 1024;
constexpr unsigned OFF_H1 = OFF_ST2 + 2048 * 28 * 8;
constexpr unsigned OFF_XF = OFF_H1 + 64 * 112 * 1024;
constexpr unsigned OFF_WQ = OFF_XF + 64 * 28 * 1024;
constexpr unsigned WS_END = OFF_WQ + 48 * 28 * 1024;

constexpr int FL_MOD = 0;
constexpr int FL_KV = 256;
constexpr int FL_O = 2304;
constexpr int FL_X1 = 4352;
constexpr int FL_H1 = 6400;
constexpr int FL_W1S = 8448;
constexpr int FL_WO = 9344;
constexpr int FL_W2 = 9600;
constexpr int FL_XF = 10496;
constexpr int FL_WQ = 10752;

DI float bf2f(u16 v) { return __uint_as_float(((unsigned)v) << 16); }
DI float bflo(unsigned v) { return __uint_as_float(v << 16); }
DI float bfhi(unsigned v) { return __uint_as_float(v & 0xffff0000u); }
DI unsigned pk2(float a, float b) {
    f32x2 v = {a, b};
    bf16x2 r = __builtin_convertvector(v, bf16x2);
    return __builtin_bit_cast(unsigned, r);
}
DI float rbf(float a) { return bflo(pk2(a, 0.f)); }
DI int kperm(int h, int j) { return 8 * (j >> 2) + 4 * h + (j & 3); }

struct Params {
    const u16* x; const u16* wqkv; const u16* wo; const u16* w1; const u16* w2; const u16* c; const u16* wmod;
    u16* out; char* ws;
};

#define TS(i)
#define TSOUT()

typedef __amdgpu_buffer_rsrc_t rsrc_t;
DI u32x4 ldb_sc1(rsrc_t r, unsigned voff, unsigned soff) { return __builtin_bit_cast(u32x4, __builtin_amdgcn_raw_buffer_load_b128(r, voff, soff, 16)); }
DI void stb_sc1(u32x4 v, rsrc_t r, unsigned voff, unsigned soff) { __builtin_amdgcn_raw_buffer_store_b128(v, r, voff + soff, 0, 16); }
DI float ldf_sc1(rsrc_t r, unsigned voff, unsigned soff) { return __builtin_bit_cast(float, __builtin_amdgcn_raw_buffer_load_b32(r, voff, soff, 16)); }
DI void stf_sc1(float v, rsrc_t r, unsigned voff, unsigned soff) { __builtin_amdgcn_raw_buffer_store_b32(__builtin_bit_cast(unsigned, v), r, voff, soff, 16); }
DI f32x2 ldf2_sc1(rsrc_t r, unsigned voff, unsigned soff) { return __builtin_bit_cast(f32x2, __builtin_amdgcn_raw_buffer_load_b64(r, voff, soff, 16)); }
DI void stf2_sc1(f32x2 v, rsrc_t r, unsigned voff, unsigned soff) { __builtin_amdgcn_raw_buffer_store_b64(__builtin_bit_cast(u32x2, v), r, voff, soff, 16); }
DI void drain() { asm volatile("s_waitcnt vmcnt(0)" ::: "memory"); }
DI void set_flag(u64* fl, int idx, int lane, u64 tag) { if (lane == 0) __hip_atomic_store(fl + idx * 16, tag, RLX_AGENT); }
#define SLP 1
DI void wait_flags(u64* fl, int idx, u64 tag) {
    bool pend = idx >= 0;
    const u64* f = fl + (pend ? idx : 0) * 16;
#pragma nounroll
    for (int spins = 0; spins < (1 << 20); spins++) {
        if (pend) pend = __hip_atomic_load(f, RLX_AGENT) != tag;
        if (!__any(pend)) break;
        __builtin_amdgcn_s_sleep(SLP);
    }
    __builtin_amdgcn_fence(__ATOMIC_ACQUIRE, "wavefront");
}
DI u32x2 trd(const char* p) { return __builtin_bit_cast(u32x2, __builtin_amdgcn_ds_read_tr16_b64_v4i16((LAS s16x4*)p)); }

constexpr int R1 = 114688;
#define LDF1(base, f) ldb_sc1(rs, l16 + ((f) & 3) * 1024, (base) + ((f) >> 2) * 4096)

DI void st_tile(char* dst, const f32x16& v) {
#pragma unroll
    for (int q = 0; q < 4; q++) *(f32x4*)(dst + q * 1024) = f32x4{v[4 * q], v[4 * q + 1], v[4 * q + 2], v[4 * q + 3]};
}
DI void add_tile(const char* src, f32x16& v) {
#pragma unroll
    for (int q = 0; q < 4; q++) {
        const f32x4 t = *(const f32x4*)(src + q * 1024);
        v[4 * q] += t[0]; v[4 * q + 1] += t[1]; v[4 * q + 2] += t[2]; v[4 * q + 3] += t[3];
    }
}
DI u32x4 pack8(const u16* v) {
    u32x4 o;
#pragma unroll
    for (int j = 0; j < 4; j++) o[j] = (unsigned)v[2 * j] | ((unsigned)v[2 * j + 1] << 16);
    return o;
}

typedef __attribute__((ext_vector_type(2))) short s16x2;
DI unsigned relu_pk(unsigned v) {
    const s16x2 z = {0, 0};
    return __builtin_bit_cast(unsigned, __builtin_elementwise_max(__builtin_bit_cast(s16x2, v), z));
}
DI f32x2 fma2(f32x2 a, f32x2 b, f32x2 c) { return __builtin_elementwise_fma(a, b, c); }
DI unsigned pk2v(f32x2 v) { return __builtin_bit_cast(unsigned, __builtin_convertvector(v, bf16x2)); }
DI f32x2 up2(unsigned v) { return f32x2{bflo(v), bfhi(v)}; }
DI f32x4 ldf4_sc1(rsrc_t r, unsigned voff, unsigned soff) { return __builtin_bit_cast(f32x4, __builtin_amdgcn_raw_buffer_load_b128(r, voff, soff, 16)); }

__global__ __launch_bounds__(512, 2) void k_main(Params p) {
    __shared__ __attribute__((aligned(16))) char smem[163840];
    const int b = blockIdx.x, g8 = b & 7, idx = b >> 3;
    const int t = threadIdx.x, lane = t & 63, w = __builtin_amdgcn_readfirstlane(t >> 6), c = lane & 31, h = lane >> 5;
    const int wm = w & 3, wh = w >> 2;
    const rsrc_t rs = __builtin_amdgcn_make_buffer_rsrc(p.ws, 0, WS_END, 0x00020000);
    u64* fl = (u64*)(p.ws + OFF_FLAGS);
    const u64 tag = (llvm_dispatch_id() + 1) * 0x9E3779B97F4A7C15ull ^ (u64)(size_t)__builtin_amdgcn_dispatch_ptr();
    const unsigned l16 = lane * 16;
    const bool gemm_blk = idx < 28;
    int tsv = 0;
    TS(0);
    const int mt2 = 4 * g8 + (gemm_blk ? idx / 7 : 0), ng = gemm_blk ? idx % 7 : 0;
    const int pn1 = 2 * g8 + (gemm_blk ? idx / 14 : 0), cg = gemm_blk ? idx % 14 : 0;
    const int pnl = 2 * g8 + (idx >> 4), head = idx & 15;
    const int mt = pnl * 4 + wm;
    char* sB = smem;
    char* sKV = smem + R1;
    char* scr = smem + R1 + w * 4096;
    const int i16 = lane & 15;

    if (b < 42) {
        float* sRed = (float*)(smem + 88 * 1024);
        u32x4 mw[7];
#pragma unroll
        for (int i = 0; i < 7; i++) mw[i] = *(const u32x4*)(p.wmod + (size_t)(56 * w + 8 * i + (lane >> 3)) * 2688 + b * 64 + (lane & 7) * 8);
        const float c0 = bf2f(p.c[56 * w + (lane < 56 ? lane : 0)]);
        SB();
        TS(50);
        drain();
        TS(51);
        const float s0 = rbf(c0 / (1.f + __expf(-c0)));
        f32x2 acc[4] = {{0.f, 0.f}, {0.f, 0.f}, {0.f, 0.f}, {0.f, 0.f}};
#pragma unroll
        for (int i = 0; i < 7; i++) {
            const float sv = __shfl(s0, 8 * i + (lane >> 3));
#pragma unroll
            for (int j = 0; j < 4; j++) acc[j] = fma2(up2(mw[i][j]), f32x2{sv, sv}, acc[j]);
        }
        *(f32x4*)(sRed + (w * 8 + (lane >> 3)) * 64 + (lane & 7) * 8) = f32x4{acc[0][0], acc[0][1], acc[1][0], acc[1][1]};
        *(f32x4*)(sRed + (w * 8 + (lane >> 3)) * 64 + (lane & 7) * 8 + 4) = f32x4{acc[2][0], acc[2][1], acc[3][0], acc[3][1]};
        __syncthreads();
        {
            float s = 0.f;
#pragma unroll
            for (int i = 0; i < 8; i++) s += sRed[(w * 8 + i) * 64 + lane];
            sRed[4096 + w * 64 + lane] = s;
        }
        __syncthreads();
        TS(52);
        if (w == 7) {
            float s = 0.f;
#pragma unroll
            for (int i = 0; i < 8; i++) s += sRed[4096 + i * 64 + lane];
            s = rbf(s);
            if ((b >= 7 && b < 14) || (b >= 28 && b < 35)) s = rbf(1.f + s);
            stf_sc1(s, rs, lane * 4, OFF_MODP + b * 256);
            TS(53);
            drain();
            TS(54);
            set_flag(fl, FL_MOD + b, lane, tag);
        }
    }
    if (w == 0) {
        const int r = lane & 7, q = lane >> 3;
        const char* xs = (const char*)(p.x + (size_t)(b * 8 + r) * D_) + q * 16;
        u32x4 v[7];
#pragma unroll
        for (int i = 0; i < 7; i++) v[i] = *(const u32x4*)(xs + i * 128);
        SB();
        f32x2 xf[28];
        f32x2 s2 = {0.f, 0.f};
#pragma unroll
        for (int i = 0; i < 7; i++)
#pragma unroll
            for (int j = 0; j < 4; j++) { xf[i * 4 + j] = up2(v[i][j]); s2 += xf[i * 4 + j]; }
        float sum = s2[0] + s2[1];
        sum += __shfl_xor(sum, 8); sum += __shfl_xor(sum, 16); sum += __shfl_xor(sum, 32);
        const float mean = sum * (1.f / D_);
        f32x2 q2 = {0.f, 0.f};
#pragma unroll
        for (int i = 0; i < 28; i++) { xf[i] -= f32x2{mean, mean}; q2 = fma2(xf[i], xf[i], q2); }
        float m2 = q2[0] + q2[1];
        m2 += __shfl_xor(m2, 8); m2 += __shfl_xor(m2, 16); m2 += __shfl_xor(m2, 32);
        const float rstd = rsqrtf(m2 * (1.f / D_) + 1e-5f);
#pragma unroll
        for (int i = 0; i < 28; i++) xf[i] *= f32x2{rstd, rstd};
        TS(45);
        wait_flags(fl, lane < 14 ? FL_MOD + lane : -1, tag);
        TS(46);
        const unsigned dst = OFF_XF + (b >> 2) * KS_D * 1024 + (q >> 1) * 1024 + ((q & 1) * 32 + (b & 3) * 8 + r) * 16;
        f32x4 gs[7][4];
#pragma unroll
        for (int i = 0; i < 7; i++) {
            gs[i][0] = ldf4_sc1(rs, q * 32 + i * 256, OFF_MODP + 1792); gs[i][1] = ldf4_sc1(rs, q * 32 + 16 + i * 256, OFF_MODP + 1792);
            gs[i][2] = ldf4_sc1(rs, q * 32 + i * 256, OFF_MODP); gs[i][3] = ldf4_sc1(rs, q * 32 + 16 + i * 256, OFF_MODP);
        }
        SB();
#pragma unroll
        for (int i = 0; i < 7; i++) {
            u32x4 o;
            o[0] = pk2v(fma2(xf[4 * i + 0], f32x2{gs[i][0][0], gs[i][0][1]}, f32x2{gs[i][2][0], gs[i][2][1]}));
            o[1] = pk2v(fma2(xf[4 * i + 1], f32x2{gs[i][0][2], gs[i][0][3]}, f32x2{gs[i][2][2], gs[i][2][3]}));
            o[2] = pk2v(fma2(xf[4 * i + 2], f32x2{gs[i][1][0], gs[i][1][1]}, f32x2{gs[i][3][0], gs[i][3][1]}));
            o[3] = pk2v(fma2(xf[4 * i + 3], f32x2{gs[i][1][2], gs[i][1][3]}, f32x2{gs[i][3][2], gs[i][3][3]}));
            stb_sc1(o, rs, dst, i * 4096);
        }
        TS(47);
        drain();
        TS(48);
        set_flag(fl, FL_XF + b, lane, tag);
    } else if (w == 1 || w == 2) {
        const int id = (b - 42) * 2 + (w - 1);
        if (b >= 42 && id < 336) {
            const int S = id / 12, part = (id % 12) >> 2, hg = id & 3;
            const char* src = (const char*)(p.wqkv + (size_t)(16 * S) * 1344 + part * 448 + hg * 112);
            u32x4 v[4];
#pragma unroll
            for (int i = 0; i < 4; i++) {
                const int pi = lane + 64 * i, pj = pi < 224 ? pi : 0;
                v[i] = *(const u32x4*)(src + (size_t)(pj / 14) * 2688 + (pj % 14) * 16);
            }
            SB();
#pragma unroll
            for (int i = 0; i < 4; i++) {
                const int pi = lane + 64 * i;
                if (pi < 224) *(u32x4*)(scr + (pi / 14) * 240 + (pi % 14) * 16) = v[i];
            }
            const char* rp = scr + (8 * h + (i16 >> 2)) * 240 + ((lane >> 4) & 1) * 32 + (i16 & 3) * 8;
#pragma unroll
            for (int j = 0; j < 4; j++) {
                const u32x2 lo = trd(rp + j * 56), hi = trd(rp + j * 56 + 4 * 240);
                u32x4 o = {lo[0], lo[1], hi[0], hi[1]};
                if (c >= DH) o = u32x4{0, 0, 0, 0};
                stb_sc1(o, rs, l16, OFF_WQ + (((hg * 4 + j) * 3 + part) * KS_D + S) * 1024);
            }
            drain();
            set_flag(fl, FL_WQ + (hg * 3 + part) * 28 + S, lane, tag);
        }
    } else if (w < 7) {
        const int id = b * 4 + (w - 3);
        if (id < 1008) {
            const bool is_wo = id < 224;
            const int f = is_wo ? id : id - 224, S = f / 7, n2 = f % 7;
            u32x4 v[2];
#pragma unroll
            for (int i = 0; i < 2; i++) {
                const int rr = 8 * i + (lane >> 3);
                int grow;
                if (is_wo) { const int d = 16 * (rr >> 3) + 4 * (2 * (S & 1) + ((rr >> 2) & 1)) + (rr & 3); grow = (S >> 1) * DH + (d < DH ? d : 0); }
                else grow = 16 * S + rr;
                v[i] = *(const u32x4*)((is_wo ? p.wo : p.w2) + (size_t)grow * D_ + n2 * 64 + (lane & 7) * 8);
            }
            SB();
            TS(40);
            drain();
            TS(41);
#pragma unroll
            for (int i = 0; i < 2; i++) *(u32x4*)(scr + (8 * i + (lane >> 3)) * 144 + (lane & 7) * 16) = v[i];
            const char* rp = scr + (4 * h + (i16 >> 2)) * 144 + ((lane >> 4) & 1) * 32 + (i16 & 3) * 8;
#pragma unroll
            for (int j = 0; j < 2; j++) {
                const u32x2 lo = trd(rp + j * 64), hi = trd(rp + j * 64 + 8 * 144);
                u32x4 o = {lo[0], lo[1], hi[0], hi[1]};
                if (is_wo && (S & 1) == 1 && h == 1) { o[2] = 0; o[3] = 0; }
                stb_sc1(o, rs, l16, is_wo ? OFF_WO + ((2 * n2 + j) * KS_O + S) * 1024 : OFF_W2 + ((2 * n2 + j) * KS_F + S) * 1024);
            }
            TS(42);
            drain();
            TS(43);
            set_flag(fl, is_wo ? FL_WO + n2 * 32 + S : FL_W2 + n2 * 112 + S, lane, tag);
        }
    }
    TS(1);
    const int gw = b * 4 + wm;
    const bool has_task = wh == 1 && gw < 784;
    const int tnt = has_task ? gw / 14 : 0, tpart = has_task ? gw % 14 : 0;
    u32x4 tv[2];
    if (has_task) {
#pragma unroll
        for (int i = 0; i < 2; i++) tv[i] = *(const u32x4*)(p.w1 + (size_t)(32 * tpart + 16 * i + (lane >> 2)) * DFF + tnt * 32 + (lane & 3) * 8);
    }
    if (w > 0) {
        const int f0 = (w - 1) * 12;
        wait_flags(fl, lane < 12 ? FL_WQ + (head >> 2) * 84 + f0 + lane : -1, tag);
        TS(55);
#pragma unroll
        for (int i = 0; i < 12; i += 2) {
            const u32x4 b0 = LDF1(OFF_WQ + (head * 84 + f0) * 1024, i), b1 = LDF1(OFF_WQ + (head * 84 + f0) * 1024, i + 1);
            SB();
            *(u32x4*)(sB + (f0 + i) * 1024 + l16) = b0;
            *(u32x4*)(sB + (f0 + i + 1) * 1024 + l16) = b1;
            SB();
        }
    }
    TS(2);
    if (has_task) {
        wait_flags(fl, lane < 2 ? FL_MOD + lane * 7 + 21 + (tpart >> 1) : -1, tag);
        float gr[2], sr[2];
#pragma unroll
        for (int i = 0; i < 2; i++) {
            gr[i] = ldf_sc1(rs, (tpart * 32 + 16 * i + (lane >> 2)) * 4, OFF_MODP + 1792 * 4);
            sr[i] = ldf_sc1(rs, (tpart * 32 + 16 * i + (lane >> 2)) * 4, OFF_MODP + 1344 * 4);
        }
        f32x2 B[4] = {{0.f, 0.f}, {0.f, 0.f}, {0.f, 0.f}, {0.f, 0.f}}, G[4] = {{0.f, 0.f}, {0.f, 0.f}, {0.f, 0.f}, {0.f, 0.f}};
#pragma unroll
        for (int i = 0; i < 2; i++) {
            u32x4 o;
#pragma unroll
            for (int j = 0; j < 4; j++) {
                const f32x2 e = up2(tv[i][j]);
                o[j] = pk2v(e * f32x2{gr[i], gr[i]});
                G[j] += up2(o[j]);
                B[j] = fma2(e, f32x2{sr[i], sr[i]}, B[j]);
            }
            *(u32x4*)(scr + (16 * i + (lane >> 2)) * 80 + (lane & 3) * 16) = o;
        }
        const char* rp = scr + (4 * h + (i16 >> 2)) * 80 + ((lane >> 4) & 1) * 32 + (i16 & 3) * 8;
#pragma unroll
        for (int i = 0; i < 2; i++) {
            const u32x2 lo = trd(rp + i * 16 * 80), hi = trd(rp + i * 16 * 80 + 8 * 80);
            stb_sc1(u32x4{lo[0], lo[1], hi[0], hi[1]}, rs, l16, OFF_W1S + (tnt * KS_D + tpart * 2 + i) * 1024);
        }
        float* pb = (float*)scr;
        *(f32x4*)(pb + (lane >> 2) * 64 + (lane & 3) * 8) = f32x4{G[0][0], G[0][1], G[1][0], G[1][1]};
        *(f32x4*)(pb + (lane >> 2) * 64 + (lane & 3) * 8 + 4) = f32x4{G[2][0], G[2][1], G[3][0], G[3][1]};
        *(f32x4*)(pb + (lane >> 2) * 64 + 32 + (lane & 3) * 8) = f32x4{B[0][0], B[0][1], B[1][0], B[1][1]};
        *(f32x4*)(pb + (lane >> 2) * 64 + 32 + (lane & 3) * 8 + 4) = f32x4{B[2][0], B[2][1], B[3][0], B[3][1]};
        float sacc = 0.f;
#pragma unroll
        for (int i = 0; i < 16; i++) sacc += pb[i * 64 + lane];
        stf_sc1(sacc, rs, lane * 4, OFF_GB2 + (tnt * 14 + tpart) * 256);
        drain();
        set_flag(fl, FL_W1S + tnt * 16 + tpart, lane, tag);
    }
    TS(3);
    f32x16 ak = {0}, av = {0}, aq = {0};
    u32x4 wo0[8], wo1[8];
    {
        const unsigned srcA = OFF_XF + (mt * KS_D + wh * 14) * 1024;
        u32x4 a[14];
        wait_flags(fl, lane < 4 ? FL_XF + mt * 4 + lane : ((lane < 12 && gemm_blk) ? FL_WO + ng * 32 + wm * 8 + (lane - 4) : -1), tag);
        TS(4);
#pragma unroll
        for (int i = 0; i < 6; i++) a[i] = LDF1(srcA, i);
        SB();
        __syncthreads();
        TS(5);
        const char* bk = sB + (wh * 14) * 1024 + l16;
        u32x4 f[2][3];
#define LDW(i) do { _Pragma("unroll") for (int q = 0; q < 3; q++) f[(i) & 1][q] = *(const u32x4*)(bk + (q * KS_D + (i)) * 1024); } while (0)
        LDW(0);
        SB();
#pragma unroll
        for (int i = 0; i < 14; i++) {
            if (i + 1 < 14) LDW(i + 1);
            if (i + 6 < 14) a[i + 6] = LDF1(srcA, i + 6);
            if (i >= 6) {
                wo0[i - 6] = LDF1(OFF_WO + ((2 * ng) * KS_O + wm * 8) * 1024, i - 6);
                if (i >= 8 && i < 12) { wo1[2 * (i - 8)] = LDF1(OFF_WO + ((2 * ng + 1) * KS_O + wm * 8) * 1024, 2 * (i - 8)); wo1[2 * (i - 8) + 1] = LDF1(OFF_WO + ((2 * ng + 1) * KS_O + wm * 8) * 1024, 2 * (i - 8) + 1); }
            }
            SB();
            ak = MFMA(f[i & 1][1], a[i], ak);
            av = MFMA(a[i], f[i & 1][2], av);
            aq = MFMA(f[i & 1][0], a[i], aq);
            SB();
        }
    }
    TS(8);
    if (wh) st_tile(sKV + w * 4096 + l16, ak); else st_tile(sKV + w * 4096 + l16, av);
    __syncthreads();
    st_tile(sB + w * 4096 + l16, aq);
    {
        u32x4 o0, o1;
        if (wh == 0) {
            add_tile(sKV + (w ^ 4) * 4096 + l16, ak);
#pragma unroll
            for (int j = 0; j < 4; j++) { o0[j] = pk2(ak[2 * j], ak[2 * j + 1]); o1[j] = pk2(ak[8 + 2 * j], ak[8 + 2 * j + 1]); }
        } else {
            add_tile(sKV + (w ^ 4) * 4096 + l16, av);
            o0[0] = pk2(av[0], av[1]); o0[1] = pk2(av[2], av[3]); o0[2] = pk2(av[8], av[9]); o0[3] = pk2(av[10], av[11]);
            o1[0] = pk2(av[4], av[5]); o1[1] = pk2(av[6], av[7]); o1[2] = pk2(av[12], av[13]); o1[3] = pk2(av[14], av[15]);
            if (c == DH) { o0 = u32x4{0x3f803f80u, 0x3f803f80u, 0x3f803f80u, 0x3f803f80u}; o1 = o0; }
        }
        const unsigned lo = wh * 2048 + (c >> 4) * 1024 + h * 256 + (c & 15) * 16;
        stb_sc1(o0, rs, lo, OFF_KV + (head * 64 + mt) * 4096);
        stb_sc1(o1, rs, lo + 512, OFF_KV + (head * 64 + mt) * 4096);
        *(u32x4*)(sKV + (8 + wm) * 4096 + lo) = o0;
        *(u32x4*)(sKV + (8 + wm) * 4096 + lo + 512) = o1;
    }
    TS(9);
    __syncthreads();
    u32x4 qf;
    {
        const int g4 = lane >> 4, s = g4 >> 1, hq = g4 & 1, tok = wh * 16 + i16;
        const char* q0p = sB + wm * 4096 + (2 * s) * 1024 + (hq * 32 + tok) * 16;
        const f32x4 x0 = *(const f32x4*)(q0p), x1 = *(const f32x4*)(q0p + 1024);
        const f32x4 y0 = *(const f32x4*)(q0p + 16384), y1 = *(const f32x4*)(q0p + 16384 + 1024);
        drain();
        set_flag(fl, FL_KV + (head * 64 + mt) * 2 + wh, lane, tag);
        const float qs = 0.18898223650461362f * 1.4426950408889634f;
        const f32x4 z0 = (x0 + y0) * qs, z1 = (x1 + y1) * qs;
        qf[0] = pk2(z0[0], z0[1]); qf[1] = pk2(z0[2], z0[3]); qf[2] = pk2(z1[0], z1[1]); qf[3] = pk2(z1[2], z1[3]);
    }
    TS(10);
    u32x4 w1r[14];
    const unsigned w1s = OFF_W1S + ((cg * 4 + wm) * KS_D + wh * 14) * 1024;
    {
        const int kt = pnl * 4 - 8 + w;
        int fidx = -1;
        if (lane < 2) { if (kt >= 0) fidx = FL_KV + (head * 64 + kt) * 2 + lane; }
        else if (gemm_blk) {
            if (lane < 10) fidx = -1;
            else if (lane < 24) fidx = FL_W1S + (cg * 4 + wm) * 16 + (lane - 10);
            else if (lane < 38) fidx = FL_W2 + ng * 112 + w * 14 + (lane - 24);
            else if (lane < 40) fidx = FL_MOD + (lane - 38) * 21 + 14 + ng;
        }
        wait_flags(fl, fidx, tag);
        TS(11);
        if (kt >= 0) {
            u32x4 kv[4];
#pragma unroll
            for (int i = 0; i < 4; i++) kv[i] = ldb_sc1(rs, l16 + i * 1024, OFF_KV + (head * 64 + kt) * 4096);
#pragma unroll
            for (int i = 0; i < 4; i++) *(u32x4*)(sKV + w * 4096 + i * 1024 + l16) = kv[i];
        } else {
#pragma unroll
            for (int i = 0; i < 4; i++) *(u32x4*)(sKV + w * 4096 + i * 1024 + l16) = u32x4{0, 0, 0, 0};
        }
        __syncthreads();
        TS(12);
        const int T = mt;
        const char* kvb = sKV + wm * 4096 + l16;
        const int g4 = lane >> 4, ql = wh * 16 + i16;
        f32x4 s[18];
        const float NEG = -1e30f;
        u32x4 kf[3][2];
        kf[0][0] = *(const u32x4*)(kvb); kf[0][1] = *(const u32x4*)(kvb + 1024);
        kf[1][0] = *(const u32x4*)(kvb + 4096); kf[1][1] = *(const u32x4*)(kvb + 4096 + 1024);
        f32x4 mlo[2], mhi[2];
#pragma unroll
        for (int u = 0; u < 2; u++)
#pragma unroll
            for (int r = 0; r < 4; r++) {
                const bool gt = (16 * u + 4 * g4 + r) > ql;
                mlo[u][r] = gt ? 0.f : NEG; mhi[u][r] = gt ? NEG : 0.f;
            }
        SB();
#pragma unroll
        for (int i = 0; i < 9; i++) {
            if (i + 2 < 9) { kf[(i + 2) % 3][0] = *(const u32x4*)(kvb + (i + 2) * 4096); kf[(i + 2) % 3][1] = *(const u32x4*)(kvb + (i + 2) * 4096 + 1024); }
            if (i < 7) w1r[i] = ldb_sc1(rs, l16 + (i & 1) * 1024, w1s + (i >> 1) * 2048);
            SB();
            const f32x4 z = {0, 0, 0, 0};
            s[2 * i] = MFMA16(kf[i % 3][0], qf, i == 0 ? mlo[0] : (i == 8 ? mhi[0] : z));
            s[2 * i + 1] = MFMA16(kf[i % 3][1], qf, i == 0 ? mlo[1] : (i == 8 ? mhi[1] : z));
            SB();
        }
        u32x4 vf[2][2];
        vf[0][0] = *(const u32x4*)(kvb + 2048); vf[0][1] = *(const u32x4*)(kvb + 3072);
        SB();
        if (__builtin_expect(T < 8, 0)) {
#pragma unroll
            for (int i = 0; i < 8; i++)
                if (T - 8 + i < 0) { s[2 * i] = f32x4{NEG, NEG, NEG, NEG}; s[2 * i + 1] = f32x4{NEG, NEG, NEG, NEG}; asm volatile("" : "+v"(s[2 * i]), "+v"(s[2 * i + 1])); }
        }
        float m0 = fmaxf(s[0][0], s[0][1]), m1 = fmaxf(s[0][2], s[0][3]);
#pragma unroll
        for (int i = 1; i < 18; i++) { m0 = fmaxf(fmaxf(m0, s[i][0]), s[i][1]); m1 = fmaxf(fmaxf(m1, s[i][2]), s[i][3]); }
        float m = fmaxf(m0, m1);
        m = fmaxf(m, __shfl_xor(m, 16));
        m = fmaxf(m, __shfl_xor(m, 32));
        f32x4 o0 = {0, 0, 0, 0}, o1 = {0, 0, 0, 0};
        SB();
#pragma unroll
        for (int i = 0; i < 9; i++) {
            if (i + 1 < 9) { vf[(i + 1) & 1][0] = *(const u32x4*)(kvb + (i + 1) * 4096 + 2048); vf[(i + 1) & 1][1] = *(const u32x4*)(kvb + (i + 1) * 4096 + 3072); }
            if (i < 7) w1r[7 + i] = ldb_sc1(rs, l16 + ((7 + i) & 1) * 1024, w1s + ((7 + i) >> 1) * 2048);
            u32x4 pf;
            pf[0] = pk2(__builtin_amdgcn_exp2f(s[2 * i][0] - m), __builtin_amdgcn_exp2f(s[2 * i][1] - m));
            pf[1] = pk2(__builtin_amdgcn_exp2f(s[2 * i][2] - m), __builtin_amdgcn_exp2f(s[2 * i][3] - m));
            pf[2] = pk2(__builtin_amdgcn_exp2f(s[2 * i + 1][0] - m), __builtin_amdgcn_exp2f(s[2 * i + 1][1] - m));
            pf[3] = pk2(__builtin_amdgcn_exp2f(s[2 * i + 1][2] - m), __builtin_amdgcn_exp2f(s[2 * i + 1][3] - m));
            SB();
            o0 = MFMA16(vf[i & 1][0], pf, o0);
            o1 = MFMA16(vf[i & 1][1], pf, o1);
            SB();
        }
        const float l = __shfl(o1[0], 48 + i16);
        const float inv = __builtin_amdgcn_rcpf(l);
        o0 *= inv; o1 *= inv;
        u32x4 ov;
        ov[0] = pk2(o0[0], o0[1]); ov[1] = pk2(o0[2], o0[3]); ov[2] = pk2(o1[0], o1[1]); ov[3] = pk2(o1[2], o1[3]);
        stb_sc1(ov, rs, (g4 >> 1) * 1024 + ((g4 & 1) * 32 + ql) * 16, OFF_O + (T * KS_O + head * 2) * 1024);
        TS(13);
        drain();
        set_flag(fl, FL_O + T * 32 + head * 2 + wh, lane, tag);
    }
    TS(14);
    if (!gemm_blk) { TSOUT(); return; }

    const int umt = 2 * mt2 + wh, unt = 2 * ng + (wm >> 1), us = wm & 1;
    u32x2 xr[2];
    f32x4 gav[2], gbv[2];
    float gbp[7];
    u32x4 w2a[14], w2b[14];
    const unsigned w2sa = OFF_W2 + ((2 * ng) * KS_F + w * 14) * 1024, w2sb = w2sa + KS_F * 1024;
#define LDW2A(i) w2a[i] = ldb_sc1(rs, l16 + ((i) & 1) * 1024, w2sa + ((i) >> 1) * 2048)
#define LDW2B(i) w2b[i] = ldb_sc1(rs, l16 + ((i) & 1) * 1024, w2sb + ((i) >> 1) * 2048)
    {
        const u16* xrow = p.x + (size_t)(umt * 32 + c) * D_ + unt * 32 + 16 * us + 4 * h;
        xr[0] = *(const u32x2*)(xrow); xr[1] = *(const u32x2*)(xrow + 8);
        const unsigned no = (unt * 32 + 16 * us) * 4;
        gav[0] = ldf4_sc1(rs, h * 16, OFF_MODP + 896 * 4 + no); gav[1] = ldf4_sc1(rs, h * 16 + 32, OFF_MODP + 896 * 4 + no);
        gbv[0] = ldf4_sc1(rs, h * 16, OFF_MODP + 2240 * 4 + no); gbv[1] = ldf4_sc1(rs, h * 16 + 32, OFF_MODP + 2240 * 4 + no);
#pragma unroll
        for (int i = 0; i < 7; i++) gbp[i] = ldf_sc1(rs, lane * 4 + i * 256, OFF_GB2 + ((cg * 4 + wm) * 14 + wh * 7) * 256);
#pragma unroll
        for (int i = 0; i < 3; i++) LDW2A(i);
        SB();
#pragma unroll
        for (int i = 0; i < 14; i++) *(u32x4*)(smem + ((wh * 14 + i) * 4 + wm) * 1024 + l16) = w1r[i];
    }
    __syncthreads();
    TS(15);
    wait_flags(fl, lane < 8 ? FL_O + umt * 32 + 8 * wm + lane : -1, tag);
    TS(16);
    u32x4 x1p;
    {
        u32x4 a0[8];
#pragma unroll
        for (int i = 0; i < 8; i++) a0[i] = LDF1(OFF_O + (umt * KS_O + wm * 8) * 1024, i);
        SB();
        f32x16 c0 = {0}, c1 = {0};
#pragma unroll
        for (int i = 0; i < 8; i++) {
            c0 = MFMA(wo0[i], a0[i], c0);
            c1 = MFMA(wo1[i], a0[i], c1);
            SB();
            if (i < 4) LDW2A(3 + i);
            SB();
        }
        TS(17);
        char* red = smem + R1 + wh * 24576;
        f32x4 r0, r1;
#define WR_HU(hu, cc, o) do { const int slot = (hu) * 3 + (wm > (hu) ? wm - 1 : wm); \
            *(f32x4*)(red + slot * 2048 + l16) = f32x4{cc[o], cc[o + 1], cc[o + 2], cc[o + 3]}; \
            *(f32x4*)(red + slot * 2048 + 1024 + l16) = f32x4{cc[o + 4], cc[o + 5], cc[o + 6], cc[o + 7]}; } while (0)
#define KP_HU(cc, o) do { r0 = f32x4{cc[o], cc[o + 1], cc[o + 2], cc[o + 3]}; r1 = f32x4{cc[o + 4], cc[o + 5], cc[o + 6], cc[o + 7]}; } while (0)
        if (wm == 0) { WR_HU(1, c0, 8); WR_HU(2, c1, 0); WR_HU(3, c1, 8); KP_HU(c0, 0); }
        else if (wm == 1) { WR_HU(0, c0, 0); WR_HU(2, c1, 0); WR_HU(3, c1, 8); KP_HU(c0, 8); }
        else if (wm == 2) { WR_HU(0, c0, 0); WR_HU(1, c0, 8); WR_HU(3, c1, 8); KP_HU(c1, 0); }
        else { WR_HU(0, c0, 0); WR_HU(1, c0, 8); WR_HU(2, c1, 0); KP_HU(c1, 8); }
        __syncthreads();
#pragma unroll
        for (int s3 = 0; s3 < 3; s3++) {
            r0 += *(const f32x4*)(red + (wm * 3 + s3) * 2048 + l16);
            r1 += *(const f32x4*)(red + (wm * 3 + s3) * 2048 + 1024 + l16);
        }
        TS(18);
        x1p[0] = pk2v(fma2(f32x2{gav[0][0], gav[0][1]}, f32x2{r0[0], r0[1]}, up2(xr[0][0])));
        x1p[1] = pk2v(fma2(f32x2{gav[0][2], gav[0][3]}, f32x2{r0[2], r0[3]}, up2(xr[0][1])));
        x1p[2] = pk2v(fma2(f32x2{gav[1][0], gav[1][1]}, f32x2{r1[0], r1[1]}, up2(xr[1][0])));
        x1p[3] = pk2v(fma2(f32x2{gav[1][2], gav[1][3]}, f32x2{r1[2], r1[3]}, up2(xr[1][1])));
        stb_sc1(x1p, rs, l16, OFF_X1 + (umt * KS_D + 2 * unt + us) * 1024);
        f32x2 xv[4];
#pragma unroll
        for (int j = 0; j < 4; j++) xv[j] = up2(x1p[j]);
        const f32x2 s2 = (xv[0] + xv[1]) + (xv[2] + xv[3]);
        float sum = s2[0] + s2[1];
        sum += __shfl_xor(sum, 32);
        const float mean = sum * (1.f / 16.f);
        f32x2 q2 = {0.f, 0.f};
#pragma unroll
        for (int j = 0; j < 4; j++) { const f32x2 d = xv[j] - f32x2{mean, mean}; q2 = fma2(d, d, q2); }
        float m2 = q2[0] + q2[1];
        m2 += __shfl_xor(m2, 32);
        if (h == 0) stf2_sc1(f32x2{mean, m2}, rs, c * 8, OFF_ST2 + (umt * 28 + 2 * unt + us) * 256);
        drain();
        set_flag(fl, FL_X1 + umt * 32 + 2 * unt + us, lane, tag);
        SB();
    }
    TS(19);
    float* sGBp = (float*)(smem + 132 * 1024);
    float* sGB = sGBp + 512;
    {
#pragma unroll
        for (int i = 7; i < 10; i++) LDW2A(i);
        SB();
        __syncthreads();
        sGBp[w * 64 + lane] = ((gbp[0] + gbp[1]) + (gbp[2] + gbp[3])) + ((gbp[4] + gbp[5]) + gbp[6]);
        __syncthreads();
        if (wh == 0) sGB[wm * 64 + lane] = sGBp[wm * 64 + lane] + sGBp[256 + wm * 64 + lane];
    }
    const int m1 = pn1 * 4 + wm;
    wait_flags(fl, lane < 28 ? FL_X1 + m1 * 32 + lane : -1, tag);
    TS(20);
    {
        constexpr int PX = 6;
        u32x4 xa[KS_D];
        f32x4 st[7];
        const unsigned xs = OFF_X1 + m1 * KS_D * 1024;
#pragma unroll
        for (int i = 0; i < PX; i++) xa[i] = LDF1(xs, i);
        SB();
        f32x16 c0 = {0}, c1 = {0};
        u32x4 fb[3][2];
        const char* bl = smem + (2 * wh) * 1024 + l16;
        const char* bl2 = bl + 57344;
#define LDFB(S) do { if ((S) < KS_D) { const char* bp_ = (S) < 14 ? bl + (S) * 4096 : bl2 + ((S) - 14) * 4096; fb[(S) % 3][0] = *(const u32x4*)(bp_); fb[(S) % 3][1] = *(const u32x4*)(bp_ + 1024); } } while (0)
        LDFB(0); LDFB(1);
        SB();
        f32x2 st2[14];
#pragma unroll
        for (int S = 0; S < KS_D; S++) {
            LDFB(S + 2);
            SB();
            c0 = MFMA(fb[S % 3][0], xa[S], c0);
            SB();
            if (S + PX < KS_D) xa[S + PX] = LDF1(xs, S + PX);
            if (S >= 8 && S < 22) st2[S - 8] = ldf2_sc1(rs, h * 3584 + c * 8 + ((S - 8) & 7) * 256, OFF_ST2 + m1 * 28 * 256 + ((S - 8) >> 3) * 2048);
            if (S >= 22 && S < 26) { LDW2A(S - 12); }
            if (S >= 26) { LDW2B(S - 26); }
            SB();
            c1 = MFMA(fb[S % 3][1], xa[S], c1);
            SB();
        }
        TS(21);
        float rsd, nmr;
        {
            float s = 0.f;
#pragma unroll
            for (int i = 0; i < 14; i++) s += st2[i][0];
            s += __shfl_xor(s, 32);
            const float mu = s * (1.f / 28.f);
            float m2 = 0.f, d2 = 0.f;
#pragma unroll
            for (int i = 0; i < 14; i++) { const float d = st2[i][0] - mu; m2 += st2[i][1]; d2 = fmaf(d, d, d2); }
            m2 = fmaf(16.f, d2, m2);
            m2 += __shfl_xor(m2, 32);
            rsd = rsqrtf(m2 * (1.f / D_) + 1e-5f);
            nmr = -mu * rsd;
        }
        __syncthreads();
        const f32x2 rs2 = {rsd, rsd}, nm2 = {nmr, nmr};
#define EPI_Q(e, n, a4, dst) do { const float* gp_ = sGB + (n) * 64 + 8 * (a4) + 4 * h; \
        const f32x4 G = *(const f32x4*)(gp_), B = *(const f32x4*)(gp_ + 32); \
        dst[((a4) & 1) * 2] = relu_pk(pk2v(fma2(rs2, f32x2{e[4 * (a4)], e[4 * (a4) + 1]}, fma2(nm2, f32x2{G[0], G[1]}, f32x2{B[0], B[1]})))); \
        dst[((a4) & 1) * 2 + 1] = relu_pk(pk2v(fma2(rs2, f32x2{e[4 * (a4) + 2], e[4 * (a4) + 3]}, fma2(nm2, f32x2{G[2], G[3]}, f32x2{B[2], B[3]})))); } while (0)
        u32x4 eo;
#pragma unroll
        for (int a4 = 0; a4 < 4; a4++) {
            EPI_Q(c0, 2 * wh, a4, eo);
            if (a4 & 1) stb_sc1(eo, rs, l16, OFF_H1 + (m1 * KS_F + (cg * 4 + 2 * wh) * 2 + (a4 >> 1)) * 1024);
        }
#pragma unroll
        for (int a4 = 0; a4 < 4; a4++) {
            EPI_Q(c1, 2 * wh + 1, a4, eo);
            if (a4 & 1) stb_sc1(eo, rs, l16, OFF_H1 + (m1 * KS_F + (cg * 4 + 2 * wh + 1) * 2 + (a4 >> 1)) * 1024);
        }
        TS(22);
        drain();
        set_flag(fl, FL_H1 + m1 * 32 + cg * 2 + wh, lane, tag);
        SB();
#pragma unroll
        for (int i = 2; i < 6; i++) LDW2B(i);
        SB();
    }
    TS(23);
    {
        const int f0 = (7 * w) >> 1, f1 = (7 * w + 6) >> 1;
        const int fi = f0 + (lane & 3);
        wait_flags(fl, (lane < 8 && fi <= f1) ? FL_H1 + (2 * mt2 + (lane >> 2)) * 32 + fi : -1, tag);
    }
    TS(24);
    f32x16 acc[4];
    {
        constexpr int PD = 4;
        u32x4 a0[14], a1[14];
        const unsigned s0 = OFF_H1 + ((2 * mt2) * KS_F + w * 14) * 1024, s1 = s0 + KS_F * 1024;
#define LDH(i) do { a0[i] = ldb_sc1(rs, l16 + ((i) & 1) * 1024, s0 + ((i) >> 1) * 2048); a1[i] = ldb_sc1(rs, l16 + ((i) & 1) * 1024, s1 + ((i) >> 1) * 2048); } while (0)
#pragma unroll
        for (int i = 0; i < PD; i++) LDH(i);
        SB();
#pragma unroll
        for (int u = 0; u < 4; u++) acc[u] = f32x16{0};
#pragma unroll
        for (int i = 0; i < 14; i++) {
            acc[0] = MFMA(w2a[i], a0[i], acc[0]);
            acc[1] = MFMA(w2b[i], a0[i], acc[1]);
            SB();
            if (i < 8) LDW2B(6 + i);
            if (i + PD < 14) LDH(i + PD);
            SB();
            acc[2] = MFMA(w2a[i], a1[i], acc[2]);
            acc[3] = MFMA(w2b[i], a1[i], acc[3]);
            SB();
        }
    }
    TS(25);
    {
        char* red = smem + w * 2048 + l16;
#pragma unroll
        for (int hu = 0; hu < 8; hu++) {
            const f32x16& cc = acc[hu >> 1];
            const int r0 = (hu & 1) * 8;
            *(f32x4*)(red + hu * 16384) = f32x4{cc[r0], cc[r0 + 1], cc[r0 + 2], cc[r0 + 3]};
            *(f32x4*)(red + hu * 16384 + 1024) = f32x4{cc[r0 + 4], cc[r0 + 5], cc[r0 + 6], cc[r0 + 7]};
        }
        __syncthreads();
        const char* rd = smem + w * 16384 + l16;
        f32x4 r0 = *(const f32x4*)(rd), r1 = *(const f32x4*)(rd + 1024);
#pragma unroll
        for (int s7 = 1; s7 < 8; s7++) { r0 += *(const f32x4*)(rd + s7 * 2048); r1 += *(const f32x4*)(rd + s7 * 2048 + 1024); }
        TS(26);
        u16* orow = p.out + (size_t)(umt * 32 + c) * D_ + unt * 32 + 16 * us + 4 * h;
        u32x2 o0, o1;
        o0[0] = pk2v(fma2(f32x2{gbv[0][0], gbv[0][1]}, f32x2{r0[0], r0[1]}, up2(x1p[0])));
        o0[1] = pk2v(fma2(f32x2{gbv[0][2], gbv[0][3]}, f32x2{r0[2], r0[3]}, up2(x1p[1])));
        o1[0] = pk2v(fma2(f32x2{gbv[1][0], gbv[1][1]}, f32x2{r1[0], r1[1]}, up2(x1p[2])));
        o1[1] = pk2v(fma2(f32x2{gbv[1][2], gbv[1][3]}, f32x2{r1[2], r1[3]}, up2(x1p[3])));
        *(u32x2*)(orow) = o0;
        *(u32x2*)(orow + 8) = o1;
    }
    TS(27); TSOUT();
}

extern "C" void kernel_launch(void* const* d_in, const int* in_sizes, int n_in,
                              void* d_out, int out_size, void* d_ws, size_t ws_size,
                              hipStream_t stream) {
    Params p;
    p.x = (const u16*)d_in[0]; p.wqkv = (const u16*)d_in[1]; p.wo = (const u16*)d_in[2];
    p.w1 = (const u16*)d_in[4]; p.w2 = (const u16*)d_in[5]; p.c = (const u16*)d_in[6]; p.wmod = (const u16*)d_in[7];
    p.out = (u16*)d_out; p.ws = (char*)d_ws;
    k_main<<<256, 512, 0, stream>>>(p);
}
```

```cpp
#include <hip/hip_runtime.h>
#include <stdint.h>

typedef __attribute__((ext_vector_type(8))) __bf16 bf16x8;
typedef __attribute__((ext_vector_type(2))) __bf16 bf16x2;
typedef __attribute__((ext_vector_type(2))) float f32x2;
typedef __attribute__((ext_vector_type(4))) float f32x4;
typedef __attribute__((ext_vector_type(16))) float f32x16;
typedef __attribute__((ext_vector_type(4))) unsigned u32x4;
typedef __attribute__((ext_vector_type(2))) unsigned u32x2;
typedef __attribute__((ext_vector_type(4))) short s16x4;
typedef unsigned short u16;
typedef unsigned long long u64;

#define DI __device__ __forceinline__
#define MFMA(a, b, c) __builtin_amdgcn_mfma_f32_32x32x16_bf16(__builtin_bit_cast(bf16x8, (a)), __builtin_bit_cast(bf16x8, (b)), (c), 0, 0, 0)
#define MFMA16(a, b, c) __builtin_amdgcn_mfma_f32_16x16x32_bf16(__builtin_bit_cast(bf16x8, (a)), __builtin_bit_cast(bf16x8, (b)), (c), 0, 0, 0)
#define SB() __builtin_amdgcn_sched_barrier(0)
#define RLX_AGENT __ATOMIC_RELAXED, __HIP_MEMORY_SCOPE_AGENT
#define LAS __attribute__((address_space(3)))

extern "C" __device__ unsigned long long llvm_dispatch_id() __asm("llvm.amdgcn.dispatch.id");

constexpr int D_ = 448, DFF = 1792, DH = 28;
constexpr int KS_D = 28, KS_O = 32, KS_F = 112;

constexpr unsigned OFF_FLAGS = 0;
constexpr unsigned OFF_MODP = 1245184;
constexpr unsigned OFF_MODG = OFF_MODP + 16384;
constexpr unsigned OFF_GB2 = OFF_MODG + 16384;
constexpr unsigned OFF_XF = OFF_GB2 + 16384;
constexpr unsigned OFF_KV = OFF_XF + 64 * 28 * 1024;
constexpr unsigned OFF_O = OFF_KV + 16 * 64 * 4096;
constexpr unsigned OFF_X1 = OFF_O + 64 * 32 * 1024;
constexpr unsigned OFF_ST2 = OFF_X1 + 64 * 28 * 1024;
constexpr unsigned OFF_H1 = OFF_ST2 + 2048 * 28 * 8;
constexpr unsigned WS_END = OFF_H1 + 64 * 112 * 1024;
constexpr unsigned OFF_W1S = OFF_XF;

constexpr int FL_MOD = 0;
constexpr int FL_GB2 = 64;
constexpr int FL_XF = 128;
constexpr int FL_KV = 384;
constexpr int FL_O = 2432;
constexpr int FL_X1 = 4480;
constexpr int FL_H1 = 6528;
constexpr int FL_ST = 8576;

DI float bf2f(u16 v) { return __uint_as_float(((unsigned)v) << 16); }
DI float bflo(unsigned v) { return __uint_as_float(v << 16); }
DI float bfhi(unsigned v) { return __uint_as_float(v & 0xffff0000u); }
DI unsigned pk2(float a, float b) {
    f32x2 v = {a, b};
    bf16x2 r = __builtin_convertvector(v, bf16x2);
    return __builtin_bit_cast(unsigned, r);
}
DI float rbf(float a) { return bflo(pk2(a, 0.f)); }
DI int kperm(int h, int j) { return 8 * (j >> 2) + 4 * h + (j & 3); }

struct Params {
    const u16* x; const u16* wqkv; const u16* wo; const u16* w1; const u16* w2; const u16* c; const u16* wmod;
    u16* out; char* ws;
};

#define TS(i)
#define TSOUT()

typedef __amdgpu_buffer_rsrc_t rsrc_t;
DI u32x4 ldb_sc1(rsrc_t r, unsigned voff, unsigned soff) { return __builtin_bit_cast(u32x4, __builtin_amdgcn_raw_buffer_load_b128(r, voff, soff, 16)); }
DI void stb_sc1(u32x4 v, rsrc_t r, unsigned voff, unsigned soff) { __builtin_amdgcn_raw_buffer_store_b128(v, r, voff + soff, 0, 16); }
DI float ldf_sc1(rsrc_t r, unsigned voff, unsigned soff) { return __builtin_bit_cast(float, __builtin_amdgcn_raw_buffer_load_b32(r, voff, soff, 16)); }
DI void stf_sc1(float v, rsrc_t r, unsigned voff, unsigned soff) { __builtin_amdgcn_raw_buffer_store_b32(__builtin_bit_cast(unsigned, v), r, voff, soff, 16); }
DI f32x2 ldf2_sc1(rsrc_t r, unsigned voff, unsigned soff) { return __builtin_bit_cast(f32x2, __builtin_amdgcn_raw_buffer_load_b64(r, voff, soff, 16)); }
DI void stf2_sc1(f32x2 v, rsrc_t r, unsigned voff, unsigned soff) { __builtin_amdgcn_raw_buffer_store_b64(__builtin_bit_cast(u32x2, v), r, voff, soff, 16); }
DI void drain() { asm volatile("s_waitcnt vmcnt(0)" ::: "memory"); }
DI void set_flag(u64* fl, int idx, int lane, u64 tag) { if (lane == 0) __hip_atomic_store(fl + idx * 16, tag, RLX_AGENT); }
#define SLP 1
DI void wait_flags(u64* fl, int idx, u64 tag) {
    bool pend = idx >= 0;
    const u64* f = fl + (pend ? idx : 0) * 16;
#pragma nounroll
    for (int spins = 0; spins < (1 << 20); spins++) {
        if (pend) pend = __hip_atomic_load(f, RLX_AGENT) != tag;
        if (!__any(pend)) break;
        __builtin_amdgcn_s_sleep(SLP);
    }
    __builtin_amdgcn_fence(__ATOMIC_ACQUIRE, "wavefront");
}
DI u32x2 trd(const char* p) { return __builtin_bit_cast(u32x2, __builtin_amdgcn_ds_read_tr16_b64_v4i16((LAS s16x4*)p)); }

constexpr int R1 = 114688;
#define LDF1(base, f) ldb_sc1(rs, l16 + ((f) & 3) * 1024, (base) + ((f) >> 2) * 4096)

DI void st_tile(char* dst, const f32x16& v) {
#pragma unroll
    for (int q = 0; q < 4; q++) *(f32x4*)(dst + q * 1024) = f32x4{v[4 * q], v[4 * q + 1], v[4 * q + 2], v[4 * q + 3]};
}
DI void add_tile(const char* src, f32x16& v) {
#pragma unroll
    for (int q = 0; q < 4; q++) {
        const f32x4 t = *(const f32x4*)(src + q * 1024);
        v[4 * q] += t[0]; v[4 * q + 1] += t[1]; v[4 * q + 2] += t[2]; v[4 * q + 3] += t[3];
    }
}
DI u32x4 pack8(const u16* v) {
    u32x4 o;
#pragma unroll
    for (int j = 0; j < 4; j++) o[j] = (unsigned)v[2 * j] | ((unsigned)v[2 * j + 1] << 16);
    return o;
}

typedef __attribute__((ext_vector_type(2))) short s16x2;
DI unsigned relu_pk(unsigned v) {
    const s16x2 z = {0, 0};
    return __builtin_bit_cast(unsigned, __builtin_elementwise_max(__builtin_bit_cast(s16x2, v), z));
}
DI f32x2 fma2(f32x2 a, f32x2 b, f32x2 c) { return __builtin_elementwise_fma(a, b, c); }
DI unsigned pk2v(f32x2 v) { return __builtin_bit_cast(unsigned, __builtin_convertvector(v, bf16x2)); }
DI f32x2 up2(unsigned v) { return f32x2{bflo(v), bfhi(v)}; }
DI f32x4 ldf4_sc1(rsrc_t r, unsigned voff, unsigned soff) { return __builtin_bit_cast(f32x4, __builtin_amdgcn_raw_buffer_load_b128(r, voff, soff, 16)); }

DI void xpose2(char* scr, int lane, u32x4& v0, u32x4& v1) {
    *(u32x4*)(scr + (lane >> 3) * 144 + (lane & 7) * 16) = v0;
    *(u32x4*)(scr + (8 + (lane >> 3)) * 144 + (lane & 7) * 16) = v1;
    const char* rp = scr + (4 * (lane >> 5) + ((lane & 15) >> 2)) * 144 + ((lane >> 4) & 1) * 32 + (lane & 3) * 8;
    const u32x2 a0 = trd(rp), a1 = trd(rp + 8 * 144), b0 = trd(rp + 64), b1 = trd(rp + 64 + 8 * 144);
    v0 = u32x4{a0[0], a0[1], a1[0], a1[1]};
    v1 = u32x4{b0[0], b0[1], b1[0], b1[1]};
}

__global__ __launch_bounds__(512, 2) void k_main(Params p) {
    __shared__ __attribute__((aligned(16))) char smem[163840];
    const int b = blockIdx.x, g8 = b & 7, idx = b >> 3;
    const int t = threadIdx.x, lane = t & 63, w = __builtin_amdgcn_readfirstlane(t >> 6), c = lane & 31, h = lane >> 5;
    const int wm = w & 3, wh = w >> 2;
    const rsrc_t rs = __builtin_amdgcn_make_buffer_rsrc(p.ws, 0, WS_END, 0x00020000);
    u64* fl = (u64*)(p.ws + OFF_FLAGS);
    const u64 tag = (llvm_dispatch_id() + 1) * 0x9E3779B97F4A7C15ull ^ (u64)(size_t)__builtin_amdgcn_dispatch_ptr();
    const unsigned l16 = lane * 16;
    const bool gemm_blk = idx < 28;
    int tsv = 0;
    TS(0);
    const int mt2 = 4 * g8 + (gemm_blk ? idx / 7 : 0), ng = gemm_blk ? idx % 7 : 0;
    const int pn1 = 2 * g8 + (gemm_blk ? idx / 14 : 0), cg = gemm_blk ? idx % 14 : 0;
    const int pnl = 2 * g8 + (idx >> 4), head = idx & 15;
    const int mt = pnl * 4 + wm;
    char* sW = smem;
    char* sKV = smem + R1;
    const int i16 = lane & 15;

    if (b < 42) {
        float* sRed = (float*)(smem + 88 * 1024);
        u32x4 mw[7];
#pragma unroll
        for (int i = 0; i < 7; i++) mw[i] = *(const u32x4*)(p.wmod + (size_t)(56 * w + 8 * i + (lane >> 3)) * 2688 + b * 64 + (lane & 7) * 8);
        const float c0 = bf2f(p.c[56 * w + (lane < 56 ? lane : 0)]);
        SB();
        const float s0 = rbf(c0 / (1.f + __expf(-c0)));
        f32x2 acc[4] = {{0.f, 0.f}, {0.f, 0.f}, {0.f, 0.f}, {0.f, 0.f}};
#pragma unroll
        for (int i = 0; i < 7; i++) {
            const float sv = __shfl(s0, 8 * i + (lane >> 3));
#pragma unroll
            for (int j = 0; j < 4; j++) acc[j] = fma2(up2(mw[i][j]), f32x2{sv, sv}, acc[j]);
        }
        *(f32x4*)(sRed + (w * 8 + (lane >> 3)) * 64 + (lane & 7) * 8) = f32x4{acc[0][0], acc[0][1], acc[1][0], acc[1][1]};
        *(f32x4*)(sRed + (w * 8 + (lane >> 3)) * 64 + (lane & 7) * 8 + 4) = f32x4{acc[2][0], acc[2][1], acc[3][0], acc[3][1]};
        float s = 0.f;
#pragma unroll
        for (int i = 0; i < 8; i++) s += sRed[(w * 8 + i) * 64 + lane];
        sRed[4096 + w * 64 + lane] = s;
        __syncthreads();
        if (w == 7) {
            s = 0.f;
#pragma unroll
            for (int i = 0; i < 8; i++) s += sRed[4096 + i * 64 + lane];
            s = rbf(s);
            if ((b >= 7 && b < 14) || (b >= 28 && b < 35)) s = rbf(1.f + s);
            if (b < 14) {
                const u64 gr = ((u64)(__float_as_uint(s) >> 16) << 48) | (tag & 0xffffffffffffull);
                __hip_atomic_store((u64*)(p.ws + OFF_MODG) + b * 64 + lane, gr, RLX_AGENT);
            } else {
                stf_sc1(s, rs, lane * 4, OFF_MODP + b * 256);
                drain();
                set_flag(fl, FL_MOD + b, lane, tag);
            }
            TS(53);
        }
        if (b < 14) __syncthreads();
    }
    if (w == 0) {
        char* scr = smem + R1;
        const int r = lane & 7, q = lane >> 3;
        const char* xs = (const char*)(p.x + (size_t)(b * 8 + r) * D_) + q * 16;
        u32x4 v[7];
#pragma unroll
        for (int i = 0; i < 7; i++) v[i] = *(const u32x4*)(xs + i * 128);
        SB();
        f32x2 xf[28];
        f32x2 s2 = {0.f, 0.f};
#pragma unroll
        for (int i = 0; i < 7; i++)
#pragma unroll
            for (int j = 0; j < 4; j++) { xf[i * 4 + j] = up2(v[i][j]); s2 += xf[i * 4 + j]; }
        float sum = s2[0] + s2[1];
        sum += __shfl_xor(sum, 8); sum += __shfl_xor(sum, 16); sum += __shfl_xor(sum, 32);
        const float mean = sum * (1.f / D_);
        f32x2 q2 = {0.f, 0.f};
#pragma unroll
        for (int i = 0; i < 28; i++) { xf[i] -= f32x2{mean, mean}; q2 = fma2(xf[i], xf[i], q2); }
        float m2 = q2[0] + q2[1];
        m2 += __shfl_xor(m2, 8); m2 += __shfl_xor(m2, 16); m2 += __shfl_xor(m2, 32);
        const float rstd = rsqrtf(m2 * (1.f / D_) + 1e-5f);
#pragma unroll
        for (int i = 0; i < 28; i++) xf[i] *= f32x2{rstd, rstd};
        TS(45);
        {
            const u64* gp = (const u64*)(p.ws + OFF_MODG) + lane;
            float* sg = (float*)scr;
#pragma nounroll
            for (int spins = 0; spins < (1 << 20); spins++) {
                const u64 g1 = __hip_atomic_load((const u64*)(p.ws + OFF_MODG) + (lane < 14 ? lane : 0) * 64 + 63, RLX_AGENT);
                if (__all(((g1 ^ tag) << 16) == 0)) break;
                __builtin_amdgcn_s_sleep(1);
            }
#pragma nounroll
            for (int spins = 0; spins < (1 << 20); spins++) {
                u64 gv[14];
#pragma unroll
                for (int i = 0; i < 14; i++) gv[i] = __hip_atomic_load(gp + i * 64, RLX_AGENT);
                bool ok = true;
#pragma unroll
                for (int i = 0; i < 14; i++) { ok = ok && ((gv[i] ^ tag) << 16) == 0; sg[i * 64 + lane] = __uint_as_float((unsigned)(gv[i] >> 32) & 0xffff0000u); }
                if (__all(ok)) break;
                __builtin_amdgcn_s_sleep(1);
            }
        }
        TS(46);
        const unsigned dst = OFF_XF + (b >> 2) * KS_D * 1024 + (q >> 1) * 1024 + ((q & 1) * 32 + (b & 3) * 8 + r) * 16;
#pragma unroll
        for (int i = 0; i < 7; i++) {
            const f32x4 g0 = *(const f32x4*)(scr + 1792 + q * 32 + i * 256), g1 = *(const f32x4*)(scr + 1792 + q * 32 + 16 + i * 256);
            const f32x4 s0 = *(const f32x4*)(scr + q * 32 + i * 256), s1 = *(const f32x4*)(scr + q * 32 + 16 + i * 256);
            u32x4 o;
            o[0] = pk2v(fma2(xf[4 * i + 0], f32x2{g0[0], g0[1]}, f32x2{s0[0], s0[1]}));
            o[1] = pk2v(fma2(xf[4 * i + 1], f32x2{g0[2], g0[3]}, f32x2{s0[2], s0[3]}));
            o[2] = pk2v(fma2(xf[4 * i + 2], f32x2{g1[0], g1[1]}, f32x2{s1[0], s1[1]}));
            o[3] = pk2v(fma2(xf[4 * i + 3], f32x2{g1[2], g1[3]}, f32x2{s1[2], s1[3]}));
            stb_sc1(o, rs, dst, i * 4096);
        }
        TS(47);
        drain();
        set_flag(fl, FL_XF + b, lane, tag);
    } else {
        const int rl = lane / 15, pc = lane % 15, part = pc / 5, pp = pc % 5;
        const int odd = head & 1;
        const unsigned col0 = part * 896 + head * 56 - odd * 8 + pp * 16;
        const char* wbase = (const char*)p.wqkv;
        char* dst = sW + (64 * (w - 1) + rl) * 192 + part * 64 + pp * 16 - odd * 8;
        {
            u32x4 v[16];
#pragma unroll
            for (int i = 0; i < 16; i++) {
                unsigned off = (unsigned)(64 * (w - 1) + i * 4 + (rl < 4 ? rl : 0)) * 2688 + col0;
                off = off < 448u * 2688u - 16u ? off : 448u * 2688u - 16u;
                v[i] = *(const u32x4*)(wbase + off);
            }
            SB();
            if (rl < 4) {
#pragma unroll
                for (int i = 0; i < 16; i++) {
                    char* d = dst + i * 4 * 192;
                    if (odd) {
                        if (pp > 0) *(u32x2*)(d) = u32x2{v[i][0], v[i][1]};
                        if (pp < 4) *(u32x2*)(d + 8) = u32x2{v[i][2], v[i][3]};
                    } else if (pp < 4) *(u32x4*)(d) = v[i];
                }
            }
            SB();
        }
    }
    TS(1);
    f32x16 ak = {0}, av = {0}, aq = {0};
    u32x4 wo0[8], wo1[8];
    {
        const unsigned srcA = OFF_XF + (mt * KS_D + wh * 14) * 1024;
        u32x4 a[14];
        wait_flags(fl, lane < 4 ? FL_XF + mt * 4 + lane : -1, tag);
        TS(4);
#pragma unroll
        for (int i = 0; i < 6; i++) a[i] = LDF1(srcA, i);
        SB();
        __syncthreads();
        TS(5);
        const char* bw = sW + (wh * 14 * 16 + 8 * h + (i16 >> 2)) * 192 + ((lane >> 4) & 1) * 32 + (i16 & 3) * 8;
        const int rr = lane >> 3;
        const u16* wop = p.wo + (size_t)(wm * 4 * DH + 4 * (rr >> 2) + (rr & 3)) * D_ + ng * 64 + (lane & 7) * 8;
        u32x2 f[2][6];
#define LDW(i) do { _Pragma("unroll") for (int q = 0; q < 3; q++) { f[(i) & 1][2 * q] = trd(bw + (i) * 3072 + q * 64); f[(i) & 1][2 * q + 1] = trd(bw + (i) * 3072 + 768 + q * 64); } } while (0)
        LDW(0);
        SB();
#pragma unroll
        for (int i = 0; i < 14; i++) {
            if (i + 1 < 14) LDW(i + 1);
            if (i + 6 < 14) a[i + 6] = LDF1(srcA, i + 6);
            if (i >= 6) {
                const int k = i - 6, hd = k >> 1, s = k & 1;
                wo0[k] = *(const u32x4*)(wop + (size_t)(hd * DH + 8 * s) * D_);
                wo1[k] = *(const u32x4*)(wop + (size_t)(hd * DH + 8 * s + ((s == 1 && rr >= 4) ? 0 : 16)) * D_);
            }
            SB();
            const u32x4 fq = {f[i & 1][0][0], f[i & 1][0][1], f[i & 1][1][0], f[i & 1][1][1]};
            const u32x4 fk = {f[i & 1][2][0], f[i & 1][2][1], f[i & 1][3][0], f[i & 1][3][1]};
            const u32x4 fv = {f[i & 1][4][0], f[i & 1][4][1], f[i & 1][5][0], f[i & 1][5][1]};
            ak = MFMA(fk, a[i], ak);
            av = MFMA(a[i], fv, av);
            aq = MFMA(fq, a[i], aq);
            SB();
        }
    }
    TS(8);
    if (wh) st_tile(sKV + w * 4096 + l16, ak); else st_tile(sKV + w * 4096 + l16, av);
    __syncthreads();
    st_tile(sW + w * 4096 + l16, aq);
    {
        u32x4 o0, o1;
        if (wh == 0) {
            add_tile(sKV + (w ^ 4) * 4096 + l16, ak);
#pragma unroll
            for (int j = 0; j < 4; j++) { o0[j] = pk2(ak[2 * j], ak[2 * j + 1]); o1[j] = pk2(ak[8 + 2 * j], ak[8 + 2 * j + 1]); }
            if (h) { o1[2] = 0; o1[3] = 0; }
        } else {
            add_tile(sKV + (w ^ 4) * 4096 + l16, av);
            o0[0] = pk2(av[0], av[1]); o0[1] = pk2(av[2], av[3]); o0[2] = pk2(av[8], av[9]); o0[3] = pk2(av[10], av[11]);
            o1[0] = pk2(av[4], av[5]); o1[1] = pk2(av[6], av[7]); o1[2] = pk2(av[12], av[13]); o1[3] = pk2(av[14], av[15]);
            if (c >= DH) { const unsigned z = c == DH ? 0x3f803f80u : 0u; o0 = u32x4{z, z, z, z}; o1 = o0; }
        }
        const unsigned lo = wh * 2048 + (c >> 4) * 1024 + h * 256 + (c & 15) * 16;
        stb_sc1(o0, rs, lo, OFF_KV + (head * 64 + mt) * 4096);
        stb_sc1(o1, rs, lo + 512, OFF_KV + (head * 64 + mt) * 4096);
        *(u32x4*)(sKV + (8 + wm) * 4096 + lo) = o0;
        *(u32x4*)(sKV + (8 + wm) * 4096 + lo + 512) = o1;
    }
    TS(9);
    __syncthreads();
    u32x4 qf;
    {
        const int g4 = lane >> 4, s = g4 >> 1, hq = g4 & 1, tok = wh * 16 + i16;
        const char* q0p = sW + wm * 4096 + (2 * s) * 1024 + (hq * 32 + tok) * 16;
        const f32x4 x0 = *(const f32x4*)(q0p), x1 = *(const f32x4*)(q0p + 1024);
        const f32x4 y0 = *(const f32x4*)(q0p + 16384), y1 = *(const f32x4*)(q0p + 16384 + 1024);
        drain();
        set_flag(fl, FL_KV + (head * 64 + mt) * 2 + wh, lane, tag);
        const float qs = 0.18898223650461362f * 1.4426950408889634f;
        const f32x4 z0 = (x0 + y0) * qs, z1 = (x1 + y1) * qs;
        qf[0] = pk2(z0[0], z0[1]); qf[1] = pk2(z0[2], z0[3]); qf[2] = pk2(z1[0], z1[1]); qf[3] = pk2(z1[2], z1[3]);
        if (g4 == 3) { qf[2] = 0; qf[3] = 0; }
    }
    {
        char* scr = smem + 40960 + w * 4096;
#pragma unroll
        for (int k = 0; k < 8; k++) {
            xpose2(scr + (k & 1) * 2304 - (k & 1) * 2304, lane, wo0[k], wo1[k]);
            if ((k & 1) && h) { wo0[k][2] = 0; wo0[k][3] = 0; wo1[k][2] = 0; wo1[k][3] = 0; }
        }
    }
    TS(10);
    u32x4 w1r[14];
    const u16* w1p = p.w1 + (size_t)(56 * w + (lane >> 4)) * DFF + cg * 128 + i16 * 8;
    {
        const int kt = pnl * 4 - 8 + w;
        wait_flags(fl, (lane < 2 && kt >= 0) ? FL_KV + (head * 64 + kt) * 2 + lane : -1, tag);
        TS(11);
        if (kt >= 0) {
            u32x4 kv[4];
#pragma unroll
            for (int i = 0; i < 4; i++) kv[i] = ldb_sc1(rs, l16 + i * 1024, OFF_KV + (head * 64 + kt) * 4096);
#pragma unroll
            for (int i = 0; i < 4; i++) *(u32x4*)(sKV + w * 4096 + i * 1024 + l16) = kv[i];
        } else {
#pragma unroll
            for (int i = 0; i < 4; i++) *(u32x4*)(sKV + w * 4096 + i * 1024 + l16) = u32x4{0, 0, 0, 0};
        }
        __syncthreads();
        TS(12);
        const int T = mt;
        const char* kvb = sKV + wm * 4096 + l16;
        const int g4 = lane >> 4, ql = wh * 16 + i16;
        f32x4 s[18];
        const float NEG = -1e30f;
        u32x4 kf[3][2];
        kf[0][0] = *(const u32x4*)(kvb); kf[0][1] = *(const u32x4*)(kvb + 1024);
        kf[1][0] = *(const u32x4*)(kvb + 4096); kf[1][1] = *(const u32x4*)(kvb + 4096 + 1024);
        f32x4 mlo[2], mhi[2];
#pragma unroll
        for (int u = 0; u < 2; u++)
#pragma unroll
            for (int r = 0; r < 4; r++) {
                const bool gt = (16 * u + 4 * g4 + r) > ql;
                mlo[u][r] = gt ? 0.f : NEG; mhi[u][r] = gt ? NEG : 0.f;
            }
        SB();
#pragma unroll
        for (int i = 0; i < 9; i++) {
            if (i + 2 < 9) { kf[(i + 2) % 3][0] = *(const u32x4*)(kvb + (i + 2) * 4096); kf[(i + 2) % 3][1] = *(const u32x4*)(kvb + (i + 2) * 4096 + 1024); }
            if (i < 7) w1r[i] = *(const u32x4*)(w1p + (size_t)(4 * i) * DFF);
            SB();
            const f32x4 z = {0, 0, 0, 0};
            s[2 * i] = MFMA16(kf[i % 3][0], qf, i == 0 ? mlo[0] : (i == 8 ? mhi[0] : z));
            s[2 * i + 1] = MFMA16(kf[i % 3][1], qf, i == 0 ? mlo[1] : (i == 8 ? mhi[1] : z));
            SB();
        }
        u32x4 vf[2][2];
        vf[0][0] = *(const u32x4*)(kvb + 2048); vf[0][1] = *(const u32x4*)(kvb + 3072);
        SB();
        if (__builtin_expect(T < 8, 0)) {
#pragma unroll
            for (int i = 0; i < 8; i++)
                if (T - 8 + i < 0) { s[2 * i] = f32x4{NEG, NEG, NEG, NEG}; s[2 * i + 1] = f32x4{NEG, NEG, NEG, NEG}; asm volatile("" : "+v"(s[2 * i]), "+v"(s[2 * i + 1])); }
        }
        float m0 = fmaxf(s[0][0], s[0][1]), m1 = fmaxf(s[0][2], s[0][3]);
#pragma unroll
        for (int i = 1; i < 18; i++) { m0 = fmaxf(fmaxf(m0, s[i][0]), s[i][1]); m1 = fmaxf(fmaxf(m1, s[i][2]), s[i][3]); }
        float m = fmaxf(m0, m1);
        m = fmaxf(m, __shfl_xor(m, 16));
        m = fmaxf(m, __shfl_xor(m, 32));
        f32x4 o0 = {0, 0, 0, 0}, o1 = {0, 0, 0, 0};
        SB();
#pragma unroll
        for (int i = 0; i < 9; i++) {
            if (i + 1 < 9) { vf[(i + 1) & 1][0] = *(const u32x4*)(kvb + (i + 1) * 4096 + 2048); vf[(i + 1) & 1][1] = *(const u32x4*)(kvb + (i + 1) * 4096 + 3072); }
            if (i < 7) w1r[7 + i] = *(const u32x4*)(w1p + (size_t)(4 * (7 + i)) * DFF);
            u32x4 pf;
            pf[0] = pk2(__builtin_amdgcn_exp2f(s[2 * i][0] - m), __builtin_amdgcn_exp2f(s[2 * i][1] - m));
            pf[1] = pk2(__builtin_amdgcn_exp2f(s[2 * i][2] - m), __builtin_amdgcn_exp2f(s[2 * i][3] - m));
            pf[2] = pk2(__builtin_amdgcn_exp2f(s[2 * i + 1][0] - m), __builtin_amdgcn_exp2f(s[2 * i + 1][1] - m));
            pf[3] = pk2(__builtin_amdgcn_exp2f(s[2 * i + 1][2] - m), __builtin_amdgcn_exp2f(s[2 * i + 1][3] - m));
            SB();
            o0 = MFMA16(vf[i & 1][0], pf, o0);
            o1 = MFMA16(vf[i & 1][1], pf, o1);
            SB();
        }
        const float l = __shfl(o1[0], 48 + i16);
        const float inv = __builtin_amdgcn_rcpf(l);
        o0 *= inv; o1 *= inv;
        u32x4 ov;
        ov[0] = pk2(o0[0], o0[1]); ov[1] = pk2(o0[2], o0[3]); ov[2] = pk2(o1[0], o1[1]); ov[3] = pk2(o1[2], o1[3]);
        stb_sc1(ov, rs, (g4 >> 1) * 1024 + ((g4 & 1) * 32 + ql) * 16, OFF_O + (T * KS_O + head * 2) * 1024);
        TS(13);
        drain();
        set_flag(fl, FL_O + T * 32 + head * 2 + wh, lane, tag);
    }
    TS(14);
    if (!gemm_blk) {
        const int gb = (idx - 28) * 8 + g8;
        if (gb < 28) {
            float* sRed = (float*)smem;
            u32x4 mw[7];
#pragma unroll
            for (int i = 0; i < 7; i++) mw[i] = *(const u32x4*)(p.w1 + (size_t)(56 * w + 8 * i + (lane >> 3)) * DFF + gb * 64 + (lane & 7) * 8);
            wait_flags(fl, lane < 14 ? FL_MOD + 21 + lane : -1, tag);
            const unsigned kk = (56 * w + (lane < 56 ? lane : 0)) * 4;
            const float gk = ldf_sc1(rs, kk, OFF_MODP + 1792 * 4), sk = ldf_sc1(rs, kk, OFF_MODP + 1344 * 4);
            f32x2 ag[4] = {{0.f, 0.f}, {0.f, 0.f}, {0.f, 0.f}, {0.f, 0.f}}, ab[4] = {{0.f, 0.f}, {0.f, 0.f}, {0.f, 0.f}, {0.f, 0.f}};
#pragma unroll
            for (int i = 0; i < 7; i++) {
                const float gv = __shfl(gk, 8 * i + (lane >> 3)), sv = __shfl(sk, 8 * i + (lane >> 3));
#pragma unroll
                for (int j = 0; j < 4; j++) { const f32x2 e = up2(mw[i][j]); ag[j] = fma2(e, f32x2{gv, gv}, ag[j]); ab[j] = fma2(e, f32x2{sv, sv}, ab[j]); }
            }
            __syncthreads();
            float* pr = sRed + (w * 8 + (lane >> 3)) * 128 + (lane & 7) * 8;
            *(f32x4*)(pr) = f32x4{ag[0][0], ag[0][1], ag[1][0], ag[1][1]}; *(f32x4*)(pr + 4) = f32x4{ag[2][0], ag[2][1], ag[3][0], ag[3][1]};
            *(f32x4*)(pr + 64) = f32x4{ab[0][0], ab[0][1], ab[1][0], ab[1][1]}; *(f32x4*)(pr + 68) = f32x4{ab[2][0], ab[2][1], ab[3][0], ab[3][1]};
            float s0 = 0.f, s1 = 0.f;
#pragma unroll
            for (int i = 0; i < 8; i++) { s0 += sRed[(w * 8 + i) * 128 + lane]; s1 += sRed[(w * 8 + i) * 128 + 64 + lane]; }
            sRed[8192 + w * 128 + lane] = s0; sRed[8192 + w * 128 + 64 + lane] = s1;
            __syncthreads();
            if (w == 0) {
                s0 = 0.f; s1 = 0.f;
#pragma unroll
                for (int i = 0; i < 8; i++) { s0 += sRed[8192 + i * 128 + lane]; s1 += sRed[8192 + i * 128 + 64 + lane]; }
                stf_sc1(s0, rs, lane * 4, OFF_GB2 + gb * 512);
                stf_sc1(s1, rs, lane * 4 + 256, OFF_GB2 + gb * 512);
                drain();
                set_flag(fl, FL_GB2 + gb, lane, tag);
            }
        }
        TSOUT();
        return;
    }

    const int umt = 2 * mt2 + wh, unt = 2 * ng + (wm >> 1), us = wm & 1;
    u32x2 xr[2];
    f32x4 gav[2], gbv[2], g2v[2];
    u32x4 w2a[14], w2b[14];
    const u16* w2p = p.w2 + (size_t)(224 * w + (lane >> 3)) * D_ + ng * 64 + (lane & 7) * 8;
#define LDW2(i) do { w2a[i] = *(const u32x4*)(w2p + (size_t)(16 * (i)) * D_); w2b[i] = *(const u32x4*)(w2p + (size_t)(16 * (i) + 8) * D_); } while (0)
    {
        const u16* xrow = p.x + (size_t)(umt * 32 + c) * D_ + unt * 32 + 16 * us + 4 * h;
        xr[0] = *(const u32x2*)(xrow); xr[1] = *(const u32x2*)(xrow + 8);
        wait_flags(fl, lane < 3 ? FL_MOD + (lane == 0 ? 14 : lane == 1 ? 28 : 35) + ng : -1, tag);
        const unsigned no = (unt * 32 + 16 * us) * 4;
        gav[0] = ldf4_sc1(rs, h * 16, OFF_MODP + 896 * 4 + no); gav[1] = ldf4_sc1(rs, h * 16 + 32, OFF_MODP + 896 * 4 + no);
        gbv[0] = ldf4_sc1(rs, h * 16, OFF_MODP + 2240 * 4 + no); gbv[1] = ldf4_sc1(rs, h * 16 + 32, OFF_MODP + 2240 * 4 + no);
        g2v[0] = ldf4_sc1(rs, h * 16, OFF_MODP + 1792 * 4 + no); g2v[1] = ldf4_sc1(rs, h * 16 + 32, OFF_MODP + 1792 * 4 + no);
        LDW2(0);
        SB();
        __syncthreads();
        char* d = smem + (56 * w + (lane >> 4)) * 256 + ((i16 ^ ((lane >> 4) << 2)) * 16);
#pragma unroll
        for (int i = 0; i < 14; i++) *(u32x4*)(d + i * 1024) = w1r[i];
    }
    TS(15);
    wait_flags(fl, lane < 8 ? FL_O + umt * 32 + 8 * wm + lane : -1, tag);
    TS(16);
    u32x4 x1p;
    {
        u32x4 a0[8];
#pragma unroll
        for (int i = 0; i < 8; i++) a0[i] = LDF1(OFF_O + (umt * KS_O + wm * 8) * 1024, i);
        SB();
        f32x16 c0 = {0}, c1 = {0};
#pragma unroll
        for (int i = 0; i < 8; i++) {
            c0 = MFMA(wo0[i], a0[i], c0);
            c1 = MFMA(wo1[i], a0[i], c1);
            SB();
            if (i == 1) LDW2(1);
            if (i == 3) LDW2(2);
            SB();
        }
        TS(17);
        char* red = smem + R1 + wh * 24576;
        f32x4 r0, r1;
#define WR_HU(hu, cc, o) do { const int slot = (hu) * 3 + (wm > (hu) ? wm - 1 : wm); \
            *(f32x4*)(red + slot * 2048 + l16) = f32x4{cc[o], cc[o + 1], cc[o + 2], cc[o + 3]}; \
            *(f32x4*)(red + slot * 2048 + 1024 + l16) = f32x4{cc[o + 4], cc[o + 5], cc[o + 6], cc[o + 7]}; } while (0)
#define KP_HU(cc, o) do { r0 = f32x4{cc[o], cc[o + 1], cc[o + 2], cc[o + 3]}; r1 = f32x4{cc[o + 4], cc[o + 5], cc[o + 6], cc[o + 7]}; } while (0)
        if (wm == 0) { WR_HU(1, c0, 8); WR_HU(2, c1, 0); WR_HU(3, c1, 8); KP_HU(c0, 0); }
        else if (wm == 1) { WR_HU(0, c0, 0); WR_HU(2, c1, 0); WR_HU(3, c1, 8); KP_HU(c0, 8); }
        else if (wm == 2) { WR_HU(0, c0, 0); WR_HU(1, c0, 8); WR_HU(3, c1, 8); KP_HU(c1, 0); }
        else { WR_HU(0, c0, 0); WR_HU(1, c0, 8); WR_HU(2, c1, 0); KP_HU(c1, 8); }
        __syncthreads();
#pragma unroll
        for (int s3 = 0; s3 < 3; s3++) {
            r0 += *(const f32x4*)(red + (wm * 3 + s3) * 2048 + l16);
            r1 += *(const f32x4*)(red + (wm * 3 + s3) * 2048 + 1024 + l16);
        }
        TS(18);
        x1p[0] = pk2v(fma2(f32x2{gav[0][0], gav[0][1]}, f32x2{r0[0], r0[1]}, up2(xr[0][0])));
        x1p[1] = pk2v(fma2(f32x2{gav[0][2], gav[0][3]}, f32x2{r0[2], r0[3]}, up2(xr[0][1])));
        x1p[2] = pk2v(fma2(f32x2{gav[1][0], gav[1][1]}, f32x2{r1[0], r1[1]}, up2(xr[1][0])));
        x1p[3] = pk2v(fma2(f32x2{gav[1][2], gav[1][3]}, f32x2{r1[2], r1[3]}, up2(xr[1][1])));
        f32x2 xv[4];
#pragma unroll
        for (int j = 0; j < 4; j++) xv[j] = up2(x1p[j]);
        u32x4 xg;
        xg[0] = pk2v(xv[0] * f32x2{g2v[0][0], g2v[0][1]}); xg[1] = pk2v(xv[1] * f32x2{g2v[0][2], g2v[0][3]});
        xg[2] = pk2v(xv[2] * f32x2{g2v[1][0], g2v[1][1]}); xg[3] = pk2v(xv[3] * f32x2{g2v[1][2], g2v[1][3]});
        stb_sc1(xg, rs, l16, OFF_X1 + (umt * KS_D + 2 * unt + us) * 1024);
        const f32x2 s2 = (xv[0] + xv[1]) + (xv[2] + xv[3]);
        float sum = s2[0] + s2[1];
        sum += __shfl_xor(sum, 32);
        const float mean = sum * (1.f / 16.f);
        f32x2 q2 = {0.f, 0.f};
#pragma unroll
        for (int j = 0; j < 4; j++) { const f32x2 d = xv[j] - f32x2{mean, mean}; q2 = fma2(d, d, q2); }
        float m2 = q2[0] + q2[1];
        m2 += __shfl_xor(m2, 32);
        if (h == 0) *(f32x2*)(red + wm * 6144 + c * 8) = f32x2{mean, m2};
        drain();
        set_flag(fl, FL_X1 + umt * 32 + 2 * unt + us, lane, tag);
        SB();
    }
    TS(19);
    char* scr2 = smem + R1 + w * 6144 + 1024;
    LDW2(3); LDW2(4);
    SB();
    __syncthreads();
    float* sGB = (float*)(smem + R1 + wh * 24576 + 6144 + 4096);
    if (wm == 0) {
        const f32x2* sp = (const f32x2*)(smem + R1 + wh * 24576) + c;
        const f32x2 a0 = sp[0], a1 = sp[768], a2 = sp[1536], a3 = sp[2304];
        const float mu = 0.25f * ((a0[0] + a1[0]) + (a2[0] + a3[0]));
        const float d0 = a0[0] - mu, d1 = a1[0] - mu, d2 = a2[0] - mu, d3 = a3[0] - mu;
        const float m2 = (a0[1] + a1[1]) + (a2[1] + a3[1]) + 16.f * ((d0 * d0 + d1 * d1) + (d2 * d2 + d3 * d3));
        if (h == 0) stf2_sc1(f32x2{mu, m2}, rs, c * 8, OFF_ST2 + (umt * 8 + ng) * 256);
        drain();
        set_flag(fl, FL_ST + umt * 8 + ng, lane, tag);
    } else if (wm == 1) {
        wait_flags(fl, lane == 0 ? FL_GB2 + cg * 2 + wh : -1, tag);
        const float g = ldf_sc1(rs, lane * 4, OFF_GB2 + (cg * 2 + wh) * 512), bb = ldf_sc1(rs, lane * 4 + 256, OFF_GB2 + (cg * 2 + wh) * 512);
        sGB[lane] = g; sGB[64 + lane] = bb;
    }
    xpose2(scr2, lane, w2a[0], w2b[0]); xpose2(scr2, lane, w2a[1], w2b[1]); xpose2(scr2, lane, w2a[2], w2b[2]);
    const int m1 = pn1 * 4 + wm;
    wait_flags(fl, lane < 28 ? FL_X1 + m1 * 32 + lane : -1, tag);
    TS(20);
    {
        constexpr int PX = 6;
        u32x4 xa[KS_D];
        const unsigned xs = OFF_X1 + m1 * KS_D * 1024;
#pragma unroll
        for (int i = 0; i < PX; i++) xa[i] = LDF1(xs, i);
        SB();
        f32x16 c0 = {0}, c1 = {0};
        u32x2 fb[3][4];
        const int q4 = i16 >> 2;
        const char* bl0 = smem + (4 * h + q4) * 256 + (((8 * wh + 2 * ((lane >> 4) & 1) + ((i16 & 3) >> 1)) ^ (q4 << 2)) * 16) + (i16 & 1) * 8;
        const char* bl1 = smem + (4 * h + q4) * 256 + (((8 * wh + 4 + 2 * ((lane >> 4) & 1) + ((i16 & 3) >> 1)) ^ (q4 << 2)) * 16) + (i16 & 1) * 8;
        const char* bh0 = bl0 + 57344; const char* bh1 = bl1 + 57344;
#define LDFB(S) do { if ((S) < KS_D) { const char* p0_ = (S) < 14 ? bl0 + (S) * 4096 : bh0 + ((S) - 14) * 4096; const char* p1_ = (S) < 14 ? bl1 + (S) * 4096 : bh1 + ((S) - 14) * 4096; \
        fb[(S) % 3][0] = trd(p0_); fb[(S) % 3][1] = trd(p0_ + 2048); fb[(S) % 3][2] = trd(p1_); fb[(S) % 3][3] = trd(p1_ + 2048); } } while (0)
        LDFB(0); LDFB(1);
        SB();
        f32x2 st2[4];
        __syncthreads();
#pragma unroll
        for (int S = 0; S < KS_D; S++) {
            LDFB(S + 2);
            SB();
            c0 = MFMA((u32x4{fb[S % 3][0][0], fb[S % 3][0][1], fb[S % 3][1][0], fb[S % 3][1][1]}), xa[S], c0);
            SB();
            if (S + PX < KS_D) xa[S + PX] = LDF1(xs, S + PX);
            if (S == 22) {
                wait_flags(fl, lane < 7 ? FL_ST + m1 * 8 + lane : -1, tag);
#pragma unroll
                for (int i = 0; i < 4; i++) st2[i] = ldf2_sc1(rs, (h * 4 + i < 7 ? h * 4 + i : 6) * 256 + c * 8, OFF_ST2 + m1 * 8 * 256);
            }
            if (S >= 23 && S < 26) { LDW2(S - 18); }
            SB();
            c1 = MFMA((u32x4{fb[S % 3][2][0], fb[S % 3][2][1], fb[S % 3][3][0], fb[S % 3][3][1]}), xa[S], c1);
            SB();
        }
        TS(21);
        float rsd, nmr;
        {
            const float wl = h ? 0.f : 1.f;
            float s = (st2[0][0] + st2[1][0]) + (st2[2][0] + wl * st2[3][0]);
            s += __shfl_xor(s, 32);
            const float mu = s * (1.f / 7.f);
            const float d0 = st2[0][0] - mu, d1 = st2[1][0] - mu, d2 = st2[2][0] - mu, d3 = st2[3][0] - mu;
            float m2 = (st2[0][1] + st2[1][1]) + (st2[2][1] + wl * st2[3][1]) + 64.f * ((d0 * d0 + d1 * d1) + (d2 * d2 + wl * d3 * d3));
            m2 += __shfl_xor(m2, 32);
            rsd = rsqrtf(m2 * (1.f / D_) + 1e-5f);
            nmr = -mu * rsd;
        }
        const f32x2 rs2 = {rsd, rsd}, nm2 = {nmr, nmr};
#define EPI_Q(e, n, a4, dst) do { \
        const f32x4 G = *(const f32x4*)(sGB + (n) * 32 + 8 * (a4) + 4 * h), B = *(const f32x4*)(sGB + 64 + (n) * 32 + 8 * (a4) + 4 * h); \
        dst[((a4) & 1) * 2] = relu_pk(pk2v(fma2(rs2, f32x2{e[4 * (a4)], e[4 * (a4) + 1]}, fma2(nm2, f32x2{G[0], G[1]}, f32x2{B[0], B[1]})))); \
        dst[((a4) & 1) * 2 + 1] = relu_pk(pk2v(fma2(rs2, f32x2{e[4 * (a4) + 2], e[4 * (a4) + 3]}, fma2(nm2, f32x2{G[2], G[3]}, f32x2{B[2], B[3]})))); } while (0)
        u32x4 eo[4];
#pragma unroll
        for (int a4 = 0; a4 < 4; a4++) { EPI_Q(c0, 0, a4, eo[a4 >> 1]); }
#pragma unroll
        for (int a4 = 0; a4 < 4; a4++) { EPI_Q(c1, 1, a4, eo[2 + (a4 >> 1)]); }
#pragma unroll
        for (int i = 0; i < 4; i++) stb_sc1(eo[i], rs, l16, OFF_H1 + (m1 * KS_F + (cg * 4 + 2 * wh) * 2 + i) * 1024);
        TS(22);
        drain();
        set_flag(fl, FL_H1 + m1 * 32 + cg * 2 + wh, lane, tag);
        SB();
        LDW2(8); LDW2(9);
        SB();
#pragma unroll
        for (int i = 3; i < 8; i++) xpose2(scr2, lane, w2a[i], w2b[i]);
    }
    TS(23);
    {
        const int f0 = (7 * w) >> 1, f1 = (7 * w + 6) >> 1;
        const int fi = f0 + (lane & 3);
        wait_flags(fl, (lane < 8 && fi <= f1) ? FL_H1 + (2 * mt2 + (lane >> 2)) * 32 + fi : -1, tag);
    }
    TS(24);
    f32x16 acc[4];
    {
        constexpr int PD = 4;
        u32x4 a0[14], a1[14];
        const unsigned s0 = OFF_H1 + ((2 * mt2) * KS_F + w * 14) * 1024, s1 = s0 + KS_F * 1024;
#define LDH(i) do { a0[i] = ldb_sc1(rs, l16 + ((i) & 1) * 1024, s0 + ((i) >> 1) * 2048); a1[i] = ldb_sc1(rs, l16 + ((i) & 1) * 1024, s1 + ((i) >> 1) * 2048); } while (0)
#pragma unroll
        for (int i = 0; i < PD; i++) LDH(i);
        SB();
        xpose2(scr2, lane, w2a[8], w2b[8]); xpose2(scr2, lane, w2a[9], w2b[9]);
        SB();
#pragma unroll
        for (int u = 0; u < 4; u++) acc[u] = f32x16{0};
#pragma unroll
        for (int i = 0; i < 14; i++) {
            acc[0] = MFMA(w2a[i], a0[i], acc[0]);
            acc[1] = MFMA(w2b[i], a0[i], acc[1]);
            SB();
            if (i < 4) LDW2(10 + i);
            if (i + PD < 14) LDH(i + PD);
            if (i >= 6 && i < 10) xpose2(scr2, lane, w2a[i + 4], w2b[i + 4]);
            SB();
            acc[2] = MFMA(w2a[i], a1[i], acc[2]);
            acc[3] = MFMA(w2b[i], a1[i], acc[3]);
            SB();
        }
    }
    TS(25);
    __syncthreads();
    {
        char* red = smem + w * 2048 + l16;
#pragma unroll
        for (int hu = 0; hu < 8; hu++) {
            const f32x16& cc = acc[hu >> 1];
            const int r0 = (hu & 1) * 8;
            *(f32x4*)(red + hu * 16384) = f32x4{cc[r0], cc[r0 + 1], cc[r0 + 2], cc[r0 + 3]};
            *(f32x4*)(red + hu * 16384 + 1024) = f32x4{cc[r0 + 4], cc[r0 + 5], cc[r0 + 6], cc[r0 + 7]};
        }
        __syncthreads();
        const char* rd = smem + w * 16384 + l16;
        f32x4 r0 = *(const f32x4*)(rd), r1 = *(const f32x4*)(rd + 1024);
#pragma unroll
        for (int s7 = 1; s7 < 8; s7++) { r0 += *(const f32x4*)(rd + s7 * 2048); r1 += *(const f32x4*)(rd + s7 * 2048 + 1024); }
        TS(26);
        u16* orow = p.out + (size_t)(umt * 32 + c) * D_ + unt * 32 + 16 * us + 4 * h;
        u32x2 o0, o1;
        o0[0] = pk2v(fma2(f32x2{gbv[0][0], gbv[0][1]}, f32x2{r0[0], r0[1]}, up2(x1p[0])));
        o0[1] = pk2v(fma2(f32x2{gbv[0][2], gbv[0][3]}, f32x2{r0[2], r0[3]}, up2(x1p[1])));
        o1[0] = pk2v(fma2(f32x2{gbv[1][0], gbv[1][1]}, f32x2{r1[0], r1[1]}, up2(x1p[2])));
        o1[1] = pk2v(fma2(f32x2{gbv[1][2], gbv[1][3]}, f32x2{r1[2], r1[3]}, up2(x1p[3])));
        *(u32x2*)(orow) = o0;
        *(u32x2*)(orow + 8) = o1;
    }
    TS(27); TSOUT();
}

extern "C" void kernel_launch(void* const* d_in, const int* in_sizes, int n_in,
                              void* d_out, int out_size, void* d_ws, size_t ws_size,
                              hipStream_t stream) {
    Params p;
    p.x = (const u16*)d_in[0]; p.wqkv = (const u16*)d_in[1]; p.wo = (const u16*)d_in[2];
    p.w1 = (const u16*)d_in[4]; p.w2 = (const u16*)d_in[5]; p.c = (const u16*)d_in[6]; p.wmod = (const u16*)d_in[7];
    p.out = (u16*)d_out; p.ws = (char*)d_ws;
    k_main<<<256, 512, 0, stream>>>(p);
}
```

```cpp
#include <hip/hip_runtime.h>
#include <stdint.h>

typedef __attribute__((ext_vector_type(8))) __bf16 bf16x8;
typedef __attribute__((ext_vector_type(2))) __bf16 bf16x2;
typedef __attribute__((ext_vector_type(2))) float f32x2;
typedef __attribute__((ext_vector_type(4))) float f32x4;
typedef __attribute__((ext_vector_type(16))) float f32x16;
typedef __attribute__((ext_vector_type(4))) unsigned u32x4;
typedef __attribute__((ext_vector_type(2))) unsigned u32x2;
typedef __attribute__((ext_vector_type(4))) short s16x4;
typedef unsigned short u16;
typedef unsigned long long u64;

#define DI __device__ __forceinline__
#define MFMA(a, b, c) __builtin_amdgcn_mfma_f32_32x32x16_bf16(__builtin_bit_cast(bf16x8, (a)), __builtin_bit_cast(bf16x8, (b)), (c), 0, 0, 0)
#define MFMA16(a, b, c) __builtin_amdgcn_mfma_f32_16x16x32_bf16(__builtin_bit_cast(bf16x8, (a)), __builtin_bit_cast(bf16x8, (b)), (c), 0, 0, 0)
#define SB() __builtin_amdgcn_sched_barrier(0)
#define RLX_AGENT __ATOMIC_RELAXED, __HIP_MEMORY_SCOPE_AGENT
#define LAS __attribute__((address_space(3)))

extern "C" __device__ unsigned long long llvm_dispatch_id() __asm("llvm.amdgcn.dispatch.id");

constexpr int D_ = 448, DFF = 1792, DH = 28;
constexpr int KS_D = 28, KS_O = 32, KS_F = 112;

constexpr unsigned OFF_FLAGS = 0;
constexpr unsigned OFF_MODP = 1245184;
constexpr unsigned OFF_MODG = OFF_MODP + 16384;
constexpr unsigned OFF_GB2 = OFF_MODG + 16384;
constexpr unsigned OFF_XF = OFF_GB2 + 16384;
constexpr unsigned OFF_KV = OFF_XF + 64 * 28 * 1024;
constexpr unsigned OFF_O = OFF_KV + 16 * 64 * 4096;
constexpr unsigned OFF_X1 = OFF_O + 64 * 32 * 1024;
constexpr unsigned OFF_ST2 = OFF_X1 + 64 * 28 * 1024;
constexpr unsigned OFF_H1 = OFF_ST2 + 2048 * 28 * 8;
constexpr unsigned WS_END = OFF_H1 + 64 * 112 * 1024;
constexpr unsigned OFF_W1S = OFF_XF;

constexpr int FL_MOD = 0;
constexpr int FL_GB2 = 64;
constexpr int FL_XF = 128;
constexpr int FL_KV = 384;
constexpr int FL_O = 2432;
constexpr int FL_X1 = 4480;
constexpr int FL_H1 = 6528;
constexpr int FL_ST = 8576;

DI float bf2f(u16 v) { return __uint_as_float(((unsigned)v) << 16); }
DI float bflo(unsigned v) { return __uint_as_float(v << 16); }
DI float bfhi(unsigned v) { return __uint_as_float(v & 0xffff0000u); }
DI unsigned pk2(float a, float b) {
    f32x2 v = {a, b};
    bf16x2 r = __builtin_convertvector(v, bf16x2);
    return __builtin_bit_cast(unsigned, r);
}
DI float rbf(float a) { return bflo(pk2(a, 0.f)); }
DI int kperm(int h, int j) { return 8 * (j >> 2) + 4 * h + (j & 3); }

struct Params {
    const u16* x; const u16* wqkv; const u16* wo; const u16* w1; const u16* w2; const u16* c; const u16* wmod;
    u16* out; char* ws;
};

#define TS(i)
#define TSOUT()

typedef __amdgpu_buffer_rsrc_t rsrc_t;
DI u32x4 ldb_sc1(rsrc_t r, unsigned voff, unsigned soff) { return __builtin_bit_cast(u32x4, __builtin_amdgcn_raw_buffer_load_b128(r, voff, soff, 16)); }
DI void stb_sc1(u32x4 v, rsrc_t r, unsigned voff, unsigned soff) { __builtin_amdgcn_raw_buffer_store_b128(v, r, voff + soff, 0, 16); }
DI float ldf_sc1(rsrc_t r, unsigned voff, unsigned soff) { return __builtin_bit_cast(float, __builtin_amdgcn_raw_buffer_load_b32(r, voff, soff, 16)); }
DI void stf_sc1(float v, rsrc_t r, unsigned voff, unsigned soff) { __builtin_amdgcn_raw_buffer_store_b32(__builtin_bit_cast(unsigned, v), r, voff, soff, 16); }
DI f32x2 ldf2_sc1(rsrc_t r, unsigned voff, unsigned soff) { return __builtin_bit_cast(f32x2, __builtin_amdgcn_raw_buffer_load_b64(r, voff, soff, 16)); }
DI void stf2_sc1(f32x2 v, rsrc_t r, unsigned voff, unsigned soff) { __builtin_amdgcn_raw_buffer_store_b64(__builtin_bit_cast(u32x2, v), r, voff, soff, 16); }
DI void drain() { asm volatile("s_waitcnt vmcnt(0)" ::: "memory"); }
DI void set_flag(u64* fl, int idx, int lane, u64 tag) { if (lane == 0) __hip_atomic_store(fl + idx * 16, tag, RLX_AGENT); }
#define SLP 1
DI void wait_flags(u64* fl, int idx, u64 tag) {
    bool pend = idx >= 0;
    const u64* f = fl + (pend ? idx : 0) * 16;
#pragma nounroll
    for (int spins = 0; spins < (1 << 20); spins++) {
        if (pend) pend = __hip_atomic_load(f, RLX_AGENT) != tag;
        if (!__any(pend)) break;
        __builtin_amdgcn_s_sleep(SLP);
    }
    __builtin_amdgcn_fence(__ATOMIC_ACQUIRE, "wavefront");
}
DI u32x2 trd(const char* p) { return __builtin_bit_cast(u32x2, __builtin_amdgcn_ds_read_tr16_b64_v4i16((LAS s16x4*)p)); }

constexpr int R1 = 114688;
#define LDF1(base, f) ldb_sc1(rs, l16 + ((f) & 3) * 1024, (base) + ((f) >> 2) * 4096)

DI void st_tile(char* dst, const f32x16& v) {
#pragma unroll
    for (int q = 0; q < 4; q++) *(f32x4*)(dst + q * 1024) = f32x4{v[4 * q], v[4 * q + 1], v[4 * q + 2], v[4 * q + 3]};
}
DI void add_tile(const char* src, f32x16& v) {
#pragma unroll
    for (int q = 0; q < 4; q++) {
        const f32x4 t = *(const f32x4*)(src + q * 1024);
        v[4 * q] += t[0]; v[4 * q + 1] += t[1]; v[4 * q + 2] += t[2]; v[4 * q + 3] += t[3];
    }
}
DI u32x4 pack8(const u16* v) {
    u32x4 o;
#pragma unroll
    for (int j = 0; j < 4; j++) o[j] = (unsigned)v[2 * j] | ((unsigned)v[2 * j + 1] << 16);
    return o;
}

typedef __attribute__((ext_vector_type(2))) short s16x2;
DI unsigned relu_pk(unsigned v) {
    const s16x2 z = {0, 0};
    return __builtin_bit_cast(unsigned, __builtin_elementwise_max(__builtin_bit_cast(s16x2, v), z));
}
DI f32x2 fma2(f32x2 a, f32x2 b, f32x2 c) { return __builtin_elementwise_fma(a, b, c); }
DI unsigned pk2v(f32x2 v) { return __builtin_bit_cast(unsigned, __builtin_convertvector(v, bf16x2)); }
DI f32x2 up2(unsigned v) { return f32x2{bflo(v), bfhi(v)}; }
DI f32x4 ldf4_sc1(rsrc_t r, unsigned voff, unsigned soff) { return __builtin_bit_cast(f32x4, __builtin_amdgcn_raw_buffer_load_b128(r, voff, soff, 16)); }

DI void xpose2(char* scr, int lane, u32x4& v0, u32x4& v1) {
    *(u32x4*)(scr + (lane >> 3) * 144 + (lane & 7) * 16) = v0;
    *(u32x4*)(scr + (8 + (lane >> 3)) * 144 + (lane & 7) * 16) = v1;
    const char* rp = scr + (4 * (lane >> 5) + ((lane & 15) >> 2)) * 144 + ((lane >> 4) & 1) * 32 + (lane & 3) * 8;
    const u32x2 a0 = trd(rp), a1 = trd(rp + 8 * 144), b0 = trd(rp + 64), b1 = trd(rp + 64 + 8 * 144);
    v0 = u32x4{a0[0], a0[1], a1[0], a1[1]};
    v1 = u32x4{b0[0], b0[1], b1[0], b1[1]};
}

__global__ __launch_bounds__(512, 2) void k_main(Params p) {
    __shared__ __attribute__((aligned(16))) char smem[163840];
    const int b = blockIdx.x, g8 = b & 7, idx = b >> 3;
    const int t = threadIdx.x, lane = t & 63, w = __builtin_amdgcn_readfirstlane(t >> 6), c = lane & 31, h = lane >> 5;
    const int wm = w & 3, wh = w >> 2;
    const rsrc_t rs = __builtin_amdgcn_make_buffer_rsrc(p.ws, 0, WS_END, 0x00020000);
    u64* fl = (u64*)(p.ws + OFF_FLAGS);
    const u64 tag = (llvm_dispatch_id() + 1) * 0x9E3779B97F4A7C15ull ^ (u64)(size_t)__builtin_amdgcn_dispatch_ptr();
    const unsigned l16 = lane * 16;
    const bool gemm_blk = idx < 28;
    int tsv = 0;
    TS(0);
    const int mt2 = 4 * g8 + (gemm_blk ? idx / 7 : 0), ng = gemm_blk ? idx % 7 : 0;
    const int pn1 = 2 * g8 + (gemm_blk ? idx / 14 : 0), cg = gemm_blk ? idx % 14 : 0;
    const int pnl = 2 * g8 + (idx >> 4), head = idx & 15;
    const int mt = pnl * 4 + wm;
    char* sW = smem;
    char* sKV = smem + R1;
    const int i16 = lane & 15;

    if (b < 42) {
        float* sRed = (float*)(smem + 88 * 1024);
        u32x4 mw[7];
#pragma unroll
        for (int i = 0; i < 7; i++) mw[i] = *(const u32x4*)(p.wmod + (size_t)(56 * w + 8 * i + (lane >> 3)) * 2688 + b * 64 + (lane & 7) * 8);
        const float c0 = bf2f(p.c[56 * w + (lane < 56 ? lane : 0)]);
        SB();
        const float s0 = rbf(c0 / (1.f + __expf(-c0)));
        f32x2 acc[4] = {{0.f, 0.f}, {0.f, 0.f}, {0.f, 0.f}, {0.f, 0.f}};
#pragma unroll
        for (int i = 0; i < 7; i++) {
            const float sv = __shfl(s0, 8 * i + (lane >> 3));
#pragma unroll
            for (int j = 0; j < 4; j++) acc[j] = fma2(up2(mw[i][j]), f32x2{sv, sv}, acc[j]);
        }
        *(f32x4*)(sRed + (w * 8 + (lane >> 3)) * 64 + (lane & 7) * 8) = f32x4{acc[0][0], acc[0][1], acc[1][0], acc[1][1]};
        *(f32x4*)(sRed + (w * 8 + (lane >> 3)) * 64 + (lane & 7) * 8 + 4) = f32x4{acc[2][0], acc[2][1], acc[3][0], acc[3][1]};
        float s = 0.f;
#pragma unroll
        for (int i = 0; i < 8; i++) s += sRed[(w * 8 + i) * 64 + lane];
        sRed[4096 + w * 64 + lane] = s;
        __syncthreads();
        if (w == 7) {
            s = 0.f;
#pragma unroll
            for (int i = 0; i < 8; i++) s += sRed[4096 + i * 64 + lane];
            s = rbf(s);
            if ((b >= 7 && b < 14) || (b >= 28 && b < 35)) s = rbf(1.f + s);
            if (b < 14) {
                const u64 gr = ((u64)(__float_as_uint(s) >> 16) << 48) | (tag & 0xffffffffffffull);
                __hip_atomic_store((u64*)(p.ws + OFF_MODG) + b * 64 + lane, gr, RLX_AGENT);
            } else {
                stf_sc1(s, rs, lane * 4, OFF_MODP + b * 256);
                drain();
                set_flag(fl, FL_MOD + b, lane, tag);
            }
            TS(53);
        }
        if (b < 14) __syncthreads();
    }
    float touch0 = 0.f;
    if (w == 0) {
        char* scr = smem + R1;
        const int r = lane & 7, q = lane >> 3;
        const char* xs = (const char*)(p.x + (size_t)(b * 8 + r) * D_) + q * 16;
        u32x4 v[7];
#pragma unroll
        for (int i = 0; i < 7; i++) v[i] = *(const u32x4*)(xs + i * 128);
        SB();
        f32x2 xf[28];
        f32x2 s2 = {0.f, 0.f};
#pragma unroll
        for (int i = 0; i < 7; i++)
#pragma unroll
            for (int j = 0; j < 4; j++) { xf[i * 4 + j] = up2(v[i][j]); s2 += xf[i * 4 + j]; }
        float sum = s2[0] + s2[1];
        sum += __shfl_xor(sum, 8); sum += __shfl_xor(sum, 16); sum += __shfl_xor(sum, 32);
        const float mean = sum * (1.f / D_);
        f32x2 q2 = {0.f, 0.f};
#pragma unroll
        for (int i = 0; i < 28; i++) { xf[i] -= f32x2{mean, mean}; q2 = fma2(xf[i], xf[i], q2); }
        float m2 = q2[0] + q2[1];
        m2 += __shfl_xor(m2, 8); m2 += __shfl_xor(m2, 16); m2 += __shfl_xor(m2, 32);
        const float rstd = rsqrtf(m2 * (1.f / D_) + 1e-5f);
#pragma unroll
        for (int i = 0; i < 28; i++) xf[i] *= f32x2{rstd, rstd};
        TS(45);
        {
            const u64* gp = (const u64*)(p.ws + OFF_MODG) + lane;
            float* sg = (float*)scr;
#pragma nounroll
            for (int spins = 0; spins < (1 << 20); spins++) {
                u64 gv[14];
#pragma unroll
                for (int i = 0; i < 14; i++) gv[i] = __hip_atomic_load(gp + i * 64, RLX_AGENT);
                bool ok = true;
#pragma unroll
                for (int i = 0; i < 14; i++) { ok = ok && ((gv[i] ^ tag) << 16) == 0; sg[i * 64 + lane] = __uint_as_float((unsigned)(gv[i] >> 32) & 0xffff0000u); }
                if (__all(ok)) break;
                __builtin_amdgcn_s_sleep(1);
            }
        }
        TS(46);
        const unsigned dst = OFF_XF + (b >> 2) * KS_D * 1024 + (q >> 1) * 1024 + ((q & 1) * 32 + (b & 3) * 8 + r) * 16;
#pragma unroll
        for (int i = 0; i < 7; i++) {
            const f32x4 g0 = *(const f32x4*)(scr + 1792 + q * 32 + i * 256), g1 = *(const f32x4*)(scr + 1792 + q * 32 + 16 + i * 256);
            const f32x4 s0 = *(const f32x4*)(scr + q * 32 + i * 256), s1 = *(const f32x4*)(scr + q * 32 + 16 + i * 256);
            u32x4 o;
            o[0] = pk2v(fma2(xf[4 * i + 0], f32x2{g0[0], g0[1]}, f32x2{s0[0], s0[1]}));
            o[1] = pk2v(fma2(xf[4 * i + 1], f32x2{g0[2], g0[3]}, f32x2{s0[2], s0[3]}));
            o[2] = pk2v(fma2(xf[4 * i + 2], f32x2{g1[0], g1[1]}, f32x2{s1[0], s1[1]}));
            o[3] = pk2v(fma2(xf[4 * i + 3], f32x2{g1[2], g1[3]}, f32x2{s1[2], s1[3]}));
            stb_sc1(o, rs, dst, i * 4096);
        }
        TS(47);
        drain();
        set_flag(fl, FL_XF + b, lane, tag);
    } else {
        const int rl = lane / 15, pc = lane % 15, part = pc / 5, pp = pc % 5;
        const int odd = head & 1;
        const unsigned col0 = part * 896 + head * 56 - odd * 8 + pp * 16;
        const char* wbase = (const char*)p.wqkv;
        char* dst = sW + (64 * (w - 1) + rl) * 192 + part * 64 + pp * 16 - odd * 8;
        {
            u32x4 v[16];
#pragma unroll
            for (int i = 0; i < 16; i++) {
                unsigned off = (unsigned)(64 * (w - 1) + i * 4 + (rl < 4 ? rl : 0)) * 2688 + col0;
                off = off < 448u * 2688u - 16u ? off : 448u * 2688u - 16u;
                v[i] = *(const u32x4*)(wbase + off);
            }
            if (w >= 6) {
                const unsigned ln = (b * 2 + (w - 6)) * 64 + lane;
                const char* tp = ln < 3136 ? (const char*)p.wo + ln * 128 : (ln < 15680 ? (const char*)p.w1 + (ln - 3136) * 128 : (const char*)p.w2 + (ln < 28224 ? ln - 15680 : 0) * 128);
                touch0 = *(const float*)tp;
            }
            SB();
            if (rl < 4) {
#pragma unroll
                for (int i = 0; i < 16; i++) {
                    char* d = dst + i * 4 * 192;
                    if (odd) {
                        if (pp > 0) *(u32x2*)(d) = u32x2{v[i][0], v[i][1]};
                        if (pp < 4) *(u32x2*)(d + 8) = u32x2{v[i][2], v[i][3]};
                    } else if (pp < 4) *(u32x4*)(d) = v[i];
                }
            }
            SB();
        }
    }
    TS(1);
    f32x16 ak = {0}, av = {0};
    u32x4 wo0[8], wo1[8];
    u32x4 a[14];
    const char* bw = sW + (wh * 14 * 16 + 8 * h + (i16 >> 2)) * 192 + ((lane >> 4) & 1) * 32 + (i16 & 3) * 8;
    {
        const unsigned srcA = OFF_XF + (mt * KS_D + wh * 14) * 1024;
        wait_flags(fl, lane < 4 ? FL_XF + mt * 4 + lane : -1, tag);
        TS(4);
#pragma unroll
        for (int i = 0; i < 8; i++) a[i] = LDF1(srcA, i);
        SB();
        asm volatile("" :: "v"(touch0));
        __syncthreads();
        TS(5);
        u32x2 f[3][4];
#define LDW(i) do { _Pragma("unroll") for (int q = 0; q < 2; q++) { f[(i) % 3][2 * q] = trd(bw + (i) * 3072 + 64 + q * 64); f[(i) % 3][2 * q + 1] = trd(bw + (i) * 3072 + 768 + 64 + q * 64); } } while (0)
        LDW(0); LDW(1);
        SB();
#pragma unroll
        for (int i = 0; i < 14; i++) {
            if (i + 2 < 14) LDW(i + 2);
            if (i + 8 < 14) a[i + 8] = LDF1(srcA, i + 8);
            SB();
            const u32x4 fk = {f[i % 3][0][0], f[i % 3][0][1], f[i % 3][1][0], f[i % 3][1][1]};
            const u32x4 fv = {f[i % 3][2][0], f[i % 3][2][1], f[i % 3][3][0], f[i % 3][3][1]};
            ak = MFMA(fk, a[i], ak);
            av = MFMA(a[i], fv, av);
            SB();
        }
    }
    TS(8);
    if (wh) st_tile(sKV + w * 4096 + l16, ak); else st_tile(sKV + w * 4096 + l16, av);
    __syncthreads();
    {
        u32x4 o0, o1;
        if (wh == 0) {
            add_tile(sKV + (w ^ 4) * 4096 + l16, ak);
#pragma unroll
            for (int j = 0; j < 4; j++) { o0[j] = pk2(ak[2 * j], ak[2 * j + 1]); o1[j] = pk2(ak[8 + 2 * j], ak[8 + 2 * j + 1]); }
            if (h) { o1[2] = 0; o1[3] = 0; }
        } else {
            add_tile(sKV + (w ^ 4) * 4096 + l16, av);
            o0[0] = pk2(av[0], av[1]); o0[1] = pk2(av[2], av[3]); o0[2] = pk2(av[8], av[9]); o0[3] = pk2(av[10], av[11]);
            o1[0] = pk2(av[4], av[5]); o1[1] = pk2(av[6], av[7]); o1[2] = pk2(av[12], av[13]); o1[3] = pk2(av[14], av[15]);
            if (c >= DH) { const unsigned z = c == DH ? 0x3f803f80u : 0u; o0 = u32x4{z, z, z, z}; o1 = o0; }
        }
        const unsigned lo = wh * 2048 + (c >> 4) * 1024 + h * 256 + (c & 15) * 16;
        stb_sc1(o0, rs, lo, OFF_KV + (head * 64 + mt) * 4096);
        stb_sc1(o1, rs, lo + 512, OFF_KV + (head * 64 + mt) * 4096);
        *(u32x4*)(sKV + (8 + wm) * 4096 + lo) = o0;
        *(u32x4*)(sKV + (8 + wm) * 4096 + lo + 512) = o1;
    }
    TS(9);
    __syncthreads();
    u32x4 qf;
    {
        f32x16 aq = {0};
        const int rr = lane >> 3;
        const u16* wop = p.wo + (size_t)(wm * 4 * DH + rr) * D_ + ng * 64 + (lane & 7) * 8;
        u32x2 f[3][2];
#define LDQ(i) do { f[(i) % 3][0] = trd(bw + (i) * 3072); f[(i) % 3][1] = trd(bw + (i) * 3072 + 768); } while (0)
        LDQ(0); LDQ(1);
        SB();
#pragma unroll
        for (int i = 0; i < 14; i++) {
            if (i + 2 < 14) LDQ(i + 2);
            SB();
            aq = MFMA((u32x4{f[i % 3][0][0], f[i % 3][0][1], f[i % 3][1][0], f[i % 3][1][1]}), a[i], aq);
            SB();
            if (i == 5) { drain(); set_flag(fl, FL_KV + (head * 64 + mt) * 2 + wh, lane, tag); SB(); }
            if (i >= 6) {
                const int k = i - 6, hd = k >> 1, s = k & 1;
                wo0[k] = *(const u32x4*)(wop + (size_t)(hd * DH + 8 * s) * D_);
                wo1[k] = *(const u32x4*)(wop + (size_t)(hd * DH + 8 * s + ((s == 1 && rr >= 4) ? 0 : 16)) * D_);
                SB();
            }
        }
        char* qx = smem + 88 * 1024;
        if ((c >> 4) != wh) {
#pragma unroll
            for (int q = 0; q < 4; q++) *(f32x4*)(qx + w * 2048 + q * 512 + (h * 16 + (c & 15)) * 16) = f32x4{aq[4 * q], aq[4 * q + 1], aq[4 * q + 2], aq[4 * q + 3]};
        } else {
#pragma unroll
            for (int q = 0; q < 4; q++) *(f32x4*)(sKV + w * 4096 + q * 512 + (h * 16 + (c & 15)) * 16 + 2048) = f32x4{aq[4 * q], aq[4 * q + 1], aq[4 * q + 2], aq[4 * q + 3]};
        }
        __syncthreads();
        const int g4 = lane >> 4, s = g4 >> 1, hq = g4 & 1;
        const char* p0 = qx + (w ^ 4) * 2048 + (2 * s) * 512 + (hq * 16 + i16) * 16;
        const char* p1 = sKV + w * 4096 + 2048 + (2 * s) * 512 + (hq * 16 + i16) * 16;
        const f32x4 x0 = *(const f32x4*)(p0), x1 = *(const f32x4*)(p0 + 512);
        const f32x4 y0 = *(const f32x4*)(p1), y1 = *(const f32x4*)(p1 + 512);
        const float qs = 0.18898223650461362f * 1.4426950408889634f;
        const f32x4 z0 = (x0 + y0) * qs, z1 = (x1 + y1) * qs;
        qf[0] = pk2(z0[0], z0[1]); qf[1] = pk2(z0[2], z0[3]); qf[2] = pk2(z1[0], z1[1]); qf[3] = pk2(z1[2], z1[3]);
        if (g4 == 3) { qf[2] = 0; qf[3] = 0; }
    }
    TS(10);
    u32x4 w1r[14];
    const u16* w1p = p.w1 + (size_t)(56 * w + (lane >> 4)) * DFF + cg * 128 + i16 * 8;
    {
        const int kt = pnl * 4 - 8 + w;
        wait_flags(fl, lane < 2 ? (kt >= 0 ? FL_KV + (head * 64 + kt) * 2 + lane : -1) : (lane < 5 ? FL_MOD + (lane == 2 ? 14 : lane == 3 ? 28 : 35) + ng : -1), tag);
        TS(11);
        if (kt >= 0) {
            u32x4 kv[4];
#pragma unroll
            for (int i = 0; i < 4; i++) kv[i] = ldb_sc1(rs, l16 + i * 1024, OFF_KV + (head * 64 + kt) * 4096);
#pragma unroll
            for (int i = 0; i < 4; i++) *(u32x4*)(sKV + w * 4096 + i * 1024 + l16) = kv[i];
        } else {
#pragma unroll
            for (int i = 0; i < 4; i++) *(u32x4*)(sKV + w * 4096 + i * 1024 + l16) = u32x4{0, 0, 0, 0};
        }
        __syncthreads();
        TS(12);
        const int T = mt;
        const char* kvb = sKV + wm * 4096 + l16;
        const int g4 = lane >> 4, ql = wh * 16 + i16;
        f32x4 s[18];
        const float NEG = -1e30f;
        u32x4 kf[3][2];
        kf[0][0] = *(const u32x4*)(kvb); kf[0][1] = *(const u32x4*)(kvb + 1024);
        kf[1][0] = *(const u32x4*)(kvb + 4096); kf[1][1] = *(const u32x4*)(kvb + 4096 + 1024);
        f32x4 mlo[2], mhi[2];
#pragma unroll
        for (int u = 0; u < 2; u++)
#pragma unroll
            for (int r = 0; r < 4; r++) {
                const bool gt = (16 * u + 4 * g4 + r) > ql;
                mlo[u][r] = gt ? 0.f : NEG; mhi[u][r] = gt ? NEG : 0.f;
            }
        SB();
#pragma unroll
        for (int i = 0; i < 9; i++) {
            if (i + 2 < 9) { kf[(i + 2) % 3][0] = *(const u32x4*)(kvb + (i + 2) * 4096); kf[(i + 2) % 3][1] = *(const u32x4*)(kvb + (i + 2) * 4096 + 1024); }
            if (i < 7) w1r[i] = *(const u32x4*)(w1p + (size_t)(4 * i) * DFF);
            SB();
            const f32x4 z = {0, 0, 0, 0};
            s[2 * i] = MFMA16(kf[i % 3][0], qf, i == 0 ? mlo[0] : (i == 8 ? mhi[0] : z));
            s[2 * i + 1] = MFMA16(kf[i % 3][1], qf, i == 0 ? mlo[1] : (i == 8 ? mhi[1] : z));
            SB();
        }
        u32x4 vf[2][2];
        vf[0][0] = *(const u32x4*)(kvb + 2048); vf[0][1] = *(const u32x4*)(kvb + 3072);
        SB();
        if (__builtin_expect(T < 8, 0)) {
#pragma unroll
            for (int i = 0; i < 8; i++)
                if (T - 8 + i < 0) { s[2 * i] = f32x4{NEG, NEG, NEG, NEG}; s[2 * i + 1] = f32x4{NEG, NEG, NEG, NEG}; asm volatile("" : "+v"(s[2 * i]), "+v"(s[2 * i + 1])); }
        }
        float m0 = fmaxf(s[0][0], s[0][1]), m1 = fmaxf(s[0][2], s[0][3]);
#pragma unroll
        for (int i = 1; i < 18; i++) { m0 = fmaxf(fmaxf(m0, s[i][0]), s[i][1]); m1 = fmaxf(fmaxf(m1, s[i][2]), s[i][3]); }
        float m = fmaxf(m0, m1);
        m = fmaxf(m, __shfl_xor(m, 16));
        m = fmaxf(m, __shfl_xor(m, 32));
        f32x4 o0 = {0, 0, 0, 0}, o1 = {0, 0, 0, 0};
        SB();
#pragma unroll
        for (int i = 0; i < 9; i++) {
            if (i + 1 < 9) { vf[(i + 1) & 1][0] = *(const u32x4*)(kvb + (i + 1) * 4096 + 2048); vf[(i + 1) & 1][1] = *(const u32x4*)(kvb + (i + 1) * 4096 + 3072); }
            if (i < 7) w1r[7 + i] = *(const u32x4*)(w1p + (size_t)(4 * (7 + i)) * DFF);
            u32x4 pf;
            pf[0] = pk2(__builtin_amdgcn_exp2f(s[2 * i][0] - m), __builtin_amdgcn_exp2f(s[2 * i][1] - m));
            pf[1] = pk2(__builtin_amdgcn_exp2f(s[2 * i][2] - m), __builtin_amdgcn_exp2f(s[2 * i][3] - m));
            pf[2] = pk2(__builtin_amdgcn_exp2f(s[2 * i + 1][0] - m), __builtin_amdgcn_exp2f(s[2 * i + 1][1] - m));
            pf[3] = pk2(__builtin_amdgcn_exp2f(s[2 * i + 1][2] - m), __builtin_amdgcn_exp2f(s[2 * i + 1][3] - m));
            SB();
            o0 = MFMA16(vf[i & 1][0], pf, o0);
            o1 = MFMA16(vf[i & 1][1], pf, o1);
            SB();
        }
        const float l = __shfl(o1[0], 48 + i16);
        const float inv = __builtin_amdgcn_rcpf(l);
        o0 *= inv; o1 *= inv;
        u32x4 ov;
        ov[0] = pk2(o0[0], o0[1]); ov[1] = pk2(o0[2], o0[3]); ov[2] = pk2(o1[0], o1[1]); ov[3] = pk2(o1[2], o1[3]);
        stb_sc1(ov, rs, (g4 >> 1) * 1024 + ((g4 & 1) * 32 + ql) * 16, OFF_O + (T * KS_O + head * 2) * 1024);
        TS(13);
        drain();
        set_flag(fl, FL_O + T * 32 + head * 2 + wh, lane, tag);
    }
    TS(14);
    if (!gemm_blk) {
        const int gb = (idx - 28) * 8 + g8;
        if (gb < 28) {
            float* sRed = (float*)smem;
            u32x4 mw[7];
#pragma unroll
            for (int i = 0; i < 7; i++) mw[i] = *(const u32x4*)(p.w1 + (size_t)(56 * w + 8 * i + (lane >> 3)) * DFF + gb * 64 + (lane & 7) * 8);
            wait_flags(fl, lane < 14 ? FL_MOD + 21 + lane : -1, tag);
            const unsigned kk = (56 * w + (lane < 56 ? lane : 0)) * 4;
            const float gk = ldf_sc1(rs, kk, OFF_MODP + 1792 * 4), sk = ldf_sc1(rs, kk, OFF_MODP + 1344 * 4);
            f32x2 ag[4] = {{0.f, 0.f}, {0.f, 0.f}, {0.f, 0.f}, {0.f, 0.f}}, ab[4] = {{0.f, 0.f}, {0.f, 0.f}, {0.f, 0.f}, {0.f, 0.f}};
#pragma unroll
            for (int i = 0; i < 7; i++) {
                const float gv = __shfl(gk, 8 * i + (lane >> 3)), sv = __shfl(sk, 8 * i + (lane >> 3));
#pragma unroll
                for (int j = 0; j < 4; j++) { const f32x2 e = up2(mw[i][j]); ag[j] = fma2(e, f32x2{gv, gv}, ag[j]); ab[j] = fma2(e, f32x2{sv, sv}, ab[j]); }
            }
            __syncthreads();
            float* pr = sRed + (w * 8 + (lane >> 3)) * 128 + (lane & 7) * 8;
            *(f32x4*)(pr) = f32x4{ag[0][0], ag[0][1], ag[1][0], ag[1][1]}; *(f32x4*)(pr + 4) = f32x4{ag[2][0], ag[2][1], ag[3][0], ag[3][1]};
            *(f32x4*)(pr + 64) = f32x4{ab[0][0], ab[0][1], ab[1][0], ab[1][1]}; *(f32x4*)(pr + 68) = f32x4{ab[2][0], ab[2][1], ab[3][0], ab[3][1]};
            float s0 = 0.f, s1 = 0.f;
#pragma unroll
            for (int i = 0; i < 8; i++) { s0 += sRed[(w * 8 + i) * 128 + lane]; s1 += sRed[(w * 8 + i) * 128 + 64 + lane]; }
            sRed[8192 + w * 128 + lane] = s0; sRed[8192 + w * 128 + 64 + lane] = s1;
            __syncthreads();
            if (w == 0) {
                s0 = 0.f; s1 = 0.f;
#pragma unroll
                for (int i = 0; i < 8; i++) { s0 += sRed[8192 + i * 128 + lane]; s1 += sRed[8192 + i * 128 + 64 + lane]; }
                stf_sc1(s0, rs, lane * 4, OFF_GB2 + gb * 512);
                stf_sc1(s1, rs, lane * 4 + 256, OFF_GB2 + gb * 512);
                drain();
                set_flag(fl, FL_GB2 + gb, lane, tag);
            }
        }
        TSOUT();
        return;
    }

    const int umt = 2 * mt2 + wh, unt = 2 * ng + (wm >> 1), us = wm & 1;
    u32x2 xr[2];
    f32x4 gav[2], gbv[2], g2v[2];
    u32x4 w2a[14], w2b[14];
    const u16* w2p = p.w2 + (size_t)(224 * w + (lane >> 3)) * D_ + ng * 64 + (lane & 7) * 8;
#define LDW2(i) do { w2a[i] = *(const u32x4*)(w2p + (size_t)(16 * (i)) * D_); w2b[i] = *(const u32x4*)(w2p + (size_t)(16 * (i) + 8) * D_); } while (0)
    {
        const u16* xrow = p.x + (size_t)(umt * 32 + c) * D_ + unt * 32 + 16 * us + 4 * h;
        xr[0] = *(const u32x2*)(xrow); xr[1] = *(const u32x2*)(xrow + 8);
        const unsigned no = (unt * 32 + 16 * us) * 4;
        gav[0] = ldf4_sc1(rs, h * 16, OFF_MODP + 896 * 4 + no); gav[1] = ldf4_sc1(rs, h * 16 + 32, OFF_MODP + 896 * 4 + no);
        gbv[0] = ldf4_sc1(rs, h * 16, OFF_MODP + 2240 * 4 + no); gbv[1] = ldf4_sc1(rs, h * 16 + 32, OFF_MODP + 2240 * 4 + no);
        g2v[0] = ldf4_sc1(rs, h * 16, OFF_MODP + 1792 * 4 + no); g2v[1] = ldf4_sc1(rs, h * 16 + 32, OFF_MODP + 1792 * 4 + no);
        LDW2(0);
        SB();
        char* scr = smem + 40960 + w * 4096;
#pragma unroll
        for (int k = 0; k < 8; k++) {
            xpose2(scr, lane, wo0[k], wo1[k]);
            if ((k & 1) && h) { wo0[k][2] = 0; wo0[k][3] = 0; wo1[k][2] = 0; wo1[k][3] = 0; }
        }
    }
    TS(15);
    wait_flags(fl, lane < 8 ? FL_O + umt * 32 + 8 * wm + lane : -1, tag);
    TS(16);
    u32x4 x1p;
    {
        u32x4 a0[8];
#pragma unroll
        for (int i = 0; i < 8; i++) a0[i] = LDF1(OFF_O + (umt * KS_O + wm * 8) * 1024, i);
        SB();
        f32x16 c0 = {0}, c1 = {0};
#pragma unroll
        for (int i = 0; i < 8; i++) {
            c0 = MFMA(wo0[i], a0[i], c0);
            c1 = MFMA(wo1[i], a0[i], c1);
            SB();
            if (i == 0) LDW2(1);
            if (i == 2) LDW2(2);
            if (i == 4) LDW2(3);
            if (i == 6) LDW2(4);
            SB();
        }
        TS(17);
        __syncthreads();
        char* red = smem + R1 + wh * 24576;
        f32x4 r0, r1;
#define WR_HU(hu, cc, o) do { const int slot = (hu) * 3 + (wm > (hu) ? wm - 1 : wm); \
            *(f32x4*)(red + slot * 2048 + l16) = f32x4{cc[o], cc[o + 1], cc[o + 2], cc[o + 3]}; \
            *(f32x4*)(red + slot * 2048 + 1024 + l16) = f32x4{cc[o + 4], cc[o + 5], cc[o + 6], cc[o + 7]}; } while (0)
#define KP_HU(cc, o) do { r0 = f32x4{cc[o], cc[o + 1], cc[o + 2], cc[o + 3]}; r1 = f32x4{cc[o + 4], cc[o + 5], cc[o + 6], cc[o + 7]}; } while (0)
        if (wm == 0) { WR_HU(1, c0, 8); WR_HU(2, c1, 0); WR_HU(3, c1, 8); KP_HU(c0, 0); }
        else if (wm == 1) { WR_HU(0, c0, 0); WR_HU(2, c1, 0); WR_HU(3, c1, 8); KP_HU(c0, 8); }
        else if (wm == 2) { WR_HU(0, c0, 0); WR_HU(1, c0, 8); WR_HU(3, c1, 8); KP_HU(c1, 0); }
        else { WR_HU(0, c0, 0); WR_HU(1, c0, 8); WR_HU(2, c1, 0); KP_HU(c1, 8); }
        __syncthreads();
#pragma unroll
        for (int s3 = 0; s3 < 3; s3++) {
            r0 += *(const f32x4*)(red + (wm * 3 + s3) * 2048 + l16);
            r1 += *(const f32x4*)(red + (wm * 3 + s3) * 2048 + 1024 + l16);
        }
        TS(18);
        x1p[0] = pk2v(fma2(f32x2{gav[0][0], gav[0][1]}, f32x2{r0[0], r0[1]}, up2(xr[0][0])));
        x1p[1] = pk2v(fma2(f32x2{gav[0][2], gav[0][3]}, f32x2{r0[2], r0[3]}, up2(xr[0][1])));
        x1p[2] = pk2v(fma2(f32x2{gav[1][0], gav[1][1]}, f32x2{r1[0], r1[1]}, up2(xr[1][0])));
        x1p[3] = pk2v(fma2(f32x2{gav[1][2], gav[1][3]}, f32x2{r1[2], r1[3]}, up2(xr[1][1])));
        f32x2 xv[4];
#pragma unroll
        for (int j = 0; j < 4; j++) xv[j] = up2(x1p[j]);
        u32x4 xg;
        xg[0] = pk2v(xv[0] * f32x2{g2v[0][0], g2v[0][1]}); xg[1] = pk2v(xv[1] * f32x2{g2v[0][2], g2v[0][3]});
        xg[2] = pk2v(xv[2] * f32x2{g2v[1][0], g2v[1][1]}); xg[3] = pk2v(xv[3] * f32x2{g2v[1][2], g2v[1][3]});
        stb_sc1(xg, rs, l16, OFF_X1 + (umt * KS_D + 2 * unt + us) * 1024);
        const f32x2 s2 = (xv[0] + xv[1]) + (xv[2] + xv[3]);
        float sum = s2[0] + s2[1];
        sum += __shfl_xor(sum, 32);
        const float mean = sum * (1.f / 16.f);
        f32x2 q2 = {0.f, 0.f};
#pragma unroll
        for (int j = 0; j < 4; j++) { const f32x2 d = xv[j] - f32x2{mean, mean}; q2 = fma2(d, d, q2); }
        float m2 = q2[0] + q2[1];
        m2 += __shfl_xor(m2, 32);
        if (h == 0) *(f32x2*)(red + wm * 6144 + c * 8) = f32x2{mean, m2};
        drain();
        set_flag(fl, FL_X1 + umt * 32 + 2 * unt + us, lane, tag);
        SB();
    }
    TS(19);
    char* scr2 = smem + R1 + w * 6144 + 1024;
    LDW2(5); LDW2(6); LDW2(7); LDW2(8);
    SB();
    {
        char* d = smem + (56 * w + (lane >> 4)) * 256 + ((i16 ^ ((lane >> 4) << 2)) * 16);
#pragma unroll
        for (int i = 0; i < 14; i++) *(u32x4*)(d + i * 1024) = w1r[i];
    }
    __syncthreads();
    float* sGB = (float*)(smem + R1 + wh * 24576 + 6144 + 4096);
    if (wm == 0) {
        const f32x2* sp = (const f32x2*)(smem + R1 + wh * 24576) + c;
        const f32x2 a0 = sp[0], a1 = sp[768], a2 = sp[1536], a3 = sp[2304];
        const float mu = 0.25f * ((a0[0] + a1[0]) + (a2[0] + a3[0]));
        const float d0 = a0[0] - mu, d1 = a1[0] - mu, d2 = a2[0] - mu, d3 = a3[0] - mu;
        const float m2 = (a0[1] + a1[1]) + (a2[1] + a3[1]) + 16.f * ((d0 * d0 + d1 * d1) + (d2 * d2 + d3 * d3));
        if (h == 0) stf2_sc1(f32x2{mu, m2}, rs, c * 8, OFF_ST2 + (umt * 8 + ng) * 256);
        drain();
        set_flag(fl, FL_ST + umt * 8 + ng, lane, tag);
    } else if (wm == 1) {
        wait_flags(fl, lane == 0 ? FL_GB2 + cg * 2 + wh : -1, tag);
        const float g = ldf_sc1(rs, lane * 4, OFF_GB2 + (cg * 2 + wh) * 512), bb = ldf_sc1(rs, lane * 4 + 256, OFF_GB2 + (cg * 2 + wh) * 512);
        sGB[lane] = g; sGB[64 + lane] = bb;
    }
    #pragma unroll
    for (int i = 0; i < 5; i++) xpose2(scr2, lane, w2a[i], w2b[i]);
    const int m1 = pn1 * 4 + wm;
    wait_flags(fl, lane < 28 ? FL_X1 + m1 * 32 + lane : -1, tag);
    TS(20);
    {
        constexpr int PX = 6;
        u32x4 xa[KS_D];
        const unsigned xs = OFF_X1 + m1 * KS_D * 1024;
#pragma unroll
        for (int i = 0; i < PX; i++) xa[i] = LDF1(xs, i);
        SB();
        f32x16 c0 = {0}, c1 = {0};
        u32x2 fb[3][4];
        const int q4 = i16 >> 2;
        const char* bl0 = smem + (4 * h + q4) * 256 + (((8 * wh + 2 * ((lane >> 4) & 1) + ((i16 & 3) >> 1)) ^ (q4 << 2)) * 16) + (i16 & 1) * 8;
        const char* bl1 = smem + (4 * h + q4) * 256 + (((8 * wh + 4 + 2 * ((lane >> 4) & 1) + ((i16 & 3) >> 1)) ^ (q4 << 2)) * 16) + (i16 & 1) * 8;
        const char* bh0 = bl0 + 57344; const char* bh1 = bl1 + 57344;
#define LDFB(S) do { if ((S) < KS_D) { const char* p0_ = (S) < 14 ? bl0 + (S) * 4096 : bh0 + ((S) - 14) * 4096; const char* p1_ = (S) < 14 ? bl1 + (S) * 4096 : bh1 + ((S) - 14) * 4096; \
        fb[(S) % 3][0] = trd(p0_); fb[(S) % 3][1] = trd(p0_ + 2048); fb[(S) % 3][2] = trd(p1_); fb[(S) % 3][3] = trd(p1_ + 2048); } } while (0)
        LDFB(0); LDFB(1);
        SB();
        f32x2 st2[4];
        u64 stf = 0;
        __syncthreads();
#pragma unroll
        for (int S = 0; S < KS_D; S++) {
            LDFB(S + 2);
            SB();
            c0 = MFMA((u32x4{fb[S % 3][0][0], fb[S % 3][0][1], fb[S % 3][1][0], fb[S % 3][1][1]}), xa[S], c0);
            SB();
            if (S + PX < KS_D) xa[S + PX] = LDF1(xs, S + PX);
            if (S == 14) stf = __hip_atomic_load(fl + (FL_ST + m1 * 8 + (lane < 7 ? lane : 0)) * 16, RLX_AGENT);
            if (S == 22) {
                if (!__all(stf == tag)) wait_flags(fl, lane < 7 ? FL_ST + m1 * 8 + lane : -1, tag);
#pragma unroll
                for (int i = 0; i < 4; i++) st2[i] = ldf2_sc1(rs, (h * 4 + i < 7 ? h * 4 + i : 6) * 256 + c * 8, OFF_ST2 + m1 * 8 * 256);
            }
            if (S >= 23) { LDW2(S - 14); }
            SB();
            c1 = MFMA((u32x4{fb[S % 3][2][0], fb[S % 3][2][1], fb[S % 3][3][0], fb[S % 3][3][1]}), xa[S], c1);
            SB();
        }
        TS(21);
        float rsd, nmr;
        {
            const float wl = h ? 0.f : 1.f;
            float s = (st2[0][0] + st2[1][0]) + (st2[2][0] + wl * st2[3][0]);
            s += __shfl_xor(s, 32);
            const float mu = s * (1.f / 7.f);
            const float d0 = st2[0][0] - mu, d1 = st2[1][0] - mu, d2 = st2[2][0] - mu, d3 = st2[3][0] - mu;
            float m2 = (st2[0][1] + st2[1][1]) + (st2[2][1] + wl * st2[3][1]) + 64.f * ((d0 * d0 + d1 * d1) + (d2 * d2 + wl * d3 * d3));
            m2 += __shfl_xor(m2, 32);
            rsd = rsqrtf(m2 * (1.f / D_) + 1e-5f);
            nmr = -mu * rsd;
        }
        const f32x2 rs2 = {rsd, rsd}, nm2 = {nmr, nmr};
#define EPI_Q(e, n, a4, dst) do { \
        const f32x4 G = *(const f32x4*)(sGB + (n) * 32 + 8 * (a4) + 4 * h), B = *(const f32x4*)(sGB + 64 + (n) * 32 + 8 * (a4) + 4 * h); \
        dst[((a4) & 1) * 2] = relu_pk(pk2v(fma2(rs2, f32x2{e[4 * (a4)], e[4 * (a4) + 1]}, fma2(nm2, f32x2{G[0], G[1]}, f32x2{B[0], B[1]})))); \
        dst[((a4) & 1) * 2 + 1] = relu_pk(pk2v(fma2(rs2, f32x2{e[4 * (a4) + 2], e[4 * (a4) + 3]}, fma2(nm2, f32x2{G[2], G[3]}, f32x2{B[2], B[3]})))); } while (0)
        u32x4 eo[4];
#pragma unroll
        for (int a4 = 0; a4 < 4; a4++) { EPI_Q(c0, 0, a4, eo[a4 >> 1]); }
#pragma unroll
        for (int a4 = 0; a4 < 4; a4++) { EPI_Q(c1, 1, a4, eo[2 + (a4 >> 1)]); }
#pragma unroll
        for (int i = 0; i < 4; i++) stb_sc1(eo[i], rs, l16, OFF_H1 + (m1 * KS_F + (cg * 4 + 2 * wh) * 2 + i) * 1024);
        TS(22);
        drain();
        set_flag(fl, FL_H1 + m1 * 32 + cg * 2 + wh, lane, tag);
        SB();
#pragma unroll
        for (int i = 5; i < 14; i++) xpose2(scr2, lane, w2a[i], w2b[i]);
        __syncthreads();
    }
    TS(23);
    {
        const int f0 = (7 * w) >> 1, f1 = (7 * w + 6) >> 1;
        const int fi = f0 + (lane & 3);
        wait_flags(fl, (lane < 8 && fi <= f1) ? FL_H1 + (2 * mt2 + (lane >> 2)) * 32 + fi : -1, tag);
    }
    TS(24);
    f32x16 acc[4];
    {
        constexpr int PD = 5;
        u32x4 a0[14], a1[14];
        const unsigned s0 = OFF_H1 + ((2 * mt2) * KS_F + w * 14) * 1024, s1 = s0 + KS_F * 1024;
#define LDH(i) do { a0[i] = ldb_sc1(rs, l16 + ((i) & 1) * 1024, s0 + ((i) >> 1) * 2048); a1[i] = ldb_sc1(rs, l16 + ((i) & 1) * 1024, s1 + ((i) >> 1) * 2048); } while (0)
#pragma unroll
        for (int i = 0; i < PD; i++) LDH(i);
        SB();
#pragma unroll
        for (int u = 0; u < 4; u++) acc[u] = f32x16{0};
#pragma unroll
        for (int i = 0; i < 14; i++) {
            acc[0] = MFMA(w2a[i], a0[i], acc[0]);
            acc[1] = MFMA(w2b[i], a0[i], acc[1]);
            SB();
            if (i + PD < 14) LDH(i + PD);
            SB();
            acc[2] = MFMA(w2a[i], a1[i], acc[2]);
            acc[3] = MFMA(w2b[i], a1[i], acc[3]);
            SB();
        }
    }
    TS(25);
    {
        char* red = smem + w * 2048 + l16;
#pragma unroll
        for (int hu = 0; hu < 8; hu++) {
            const f32x16& cc = acc[hu >> 1];
            const int r0 = (hu & 1) * 8;
            *(f32x4*)(red + hu * 16384) = f32x4{cc[r0], cc[r0 + 1], cc[r0 + 2], cc[r0 + 3]};
            *(f32x4*)(red + hu * 16384 + 1024) = f32x4{cc[r0 + 4], cc[r0 + 5], cc[r0 + 6], cc[r0 + 7]};
        }
        __syncthreads();
        const char* rd = smem + w * 16384 + l16;
        f32x4 r0 = *(const f32x4*)(rd), r1 = *(const f32x4*)(rd + 1024);
#pragma unroll
        for (int s7 = 1; s7 < 8; s7++) { r0 += *(const f32x4*)(rd + s7 * 2048); r1 += *(const f32x4*)(rd + s7 * 2048 + 1024); }
        TS(26);
        u16* orow = p.out + (size_t)(umt * 32 + c) * D_ + unt * 32 + 16 * us + 4 * h;
        u32x2 o0, o1;
        o0[0] = pk2v(fma2(f32x2{gbv[0][0], gbv[0][1]}, f32x2{r0[0], r0[1]}, up2(x1p[0])));
        o0[1] = pk2v(fma2(f32x2{gbv[0][2], gbv[0][3]}, f32x2{r0[2], r0[3]}, up2(x1p[1])));
        o1[0] = pk2v(fma2(f32x2{gbv[1][0], gbv[1][1]}, f32x2{r1[0], r1[1]}, up2(x1p[2])));
        o1[1] = pk2v(fma2(f32x2{gbv[1][2], gbv[1][3]}, f32x2{r1[2], r1[3]}, up2(x1p[3])));
        *(u32x2*)(orow) = o0;
        *(u32x2*)(orow + 8) = o1;
    }
    TS(27); TSOUT();
}

extern "C" void kernel_launch(void* const* d_in, const int* in_sizes, int n_in,
                              void* d_out, int out_size, void* d_ws, size_t ws_size,
                              hipStream_t stream) {
    Params p;
    p.x = (const u16*)d_in[0]; p.wqkv = (const u16*)d_in[1]; p.wo = (const u16*)d_in[2];
    p.w1 = (const u16*)d_in[4]; p.w2 = (const u16*)d_in[5]; p.c = (const u16*)d_in[6]; p.wmod = (const u16*)d_in[7];
    p.out = (u16*)d_out; p.ws = (char*)d_ws;
    k_main<<<256, 512, 0, stream>>>(p);
}
```

```cpp
#include <hip/hip_runtime.h>
#include <stdint.h>

typedef __attribute__((ext_vector_type(8))) __bf16 bf16x8;
typedef __attribute__((ext_vector_type(2))) __bf16 bf16x2;
typedef __attribute__((ext_vector_type(2))) float f32x2;
typedef __attribute__((ext_vector_type(4))) float f32x4;
typedef __attribute__((ext_vector_type(16))) float f32x16;
typedef __attribute__((ext_vector_type(4))) unsigned u32x4;
typedef __attribute__((ext_vector_type(2))) unsigned u32x2;
typedef __attribute__((ext_vector_type(4))) short s16x4;
typedef unsigned short u16;
typedef unsigned long long u64;

#define DI __device__ __forceinline__
#define MFMA(a, b, c) __builtin_amdgcn_mfma_f32_32x32x16_bf16(__builtin_bit_cast(bf16x8, (a)), __builtin_bit_cast(bf16x8, (b)), (c), 0, 0, 0)
#define MFMA16(a, b, c) __builtin_amdgcn_mfma_f32_16x16x32_bf16(__builtin_bit_cast(bf16x8, (a)), __builtin_bit_cast(bf16x8, (b)), (c), 0, 0, 0)
#define SB() __builtin_amdgcn_sched_barrier(0)
#define RLX_AGENT __ATOMIC_RELAXED, __HIP_MEMORY_SCOPE_AGENT
#define LAS __attribute__((address_space(3)))

extern "C" __device__ unsigned long long llvm_dispatch_id() __asm("llvm.amdgcn.dispatch.id");

constexpr int D_ = 448, DFF = 1792, DH = 28;
constexpr int KS_D = 28, KS_O = 32, KS_F = 112;

constexpr unsigned OFF_FLAGS = 0;
constexpr unsigned OFF_MODP = 1245184;
constexpr unsigned OFF_MODG = OFF_MODP + 16384;
constexpr unsigned OFF_GB2 = OFF_MODG + 16384;
constexpr unsigned OFF_XF = OFF_GB2 + 16384;
constexpr unsigned OFF_KV = OFF_XF + 64 * 28 * 1024;
constexpr unsigned OFF_O = OFF_KV + 16 * 64 * 4096;
constexpr unsigned OFF_X1 = OFF_O + 64 * 32 * 1024;
constexpr unsigned OFF_ST2 = OFF_X1 + 64 * 28 * 1024;
constexpr unsigned OFF_H1 = OFF_ST2 + 2048 * 28 * 8;
constexpr unsigned WS_END = OFF_H1 + 64 * 112 * 1024;
constexpr unsigned OFF_W1S = OFF_XF;

constexpr int FL_MOD = 0;
constexpr int FL_GB2 = 64;
constexpr int FL_XF = 128;
constexpr int FL_KV = 384;
constexpr int FL_O = 2432;
constexpr int FL_X1 = 4480;
constexpr int FL_H1 = 6528;
constexpr int FL_ST = 8576;

DI float bf2f(u16 v) { return __uint_as_float(((unsigned)v) << 16); }
DI float bflo(unsigned v) { return __uint_as_float(v << 16); }
DI float bfhi(unsigned v) { return __uint_as_float(v & 0xffff0000u); }
DI unsigned pk2(float a, float b) {
    f32x2 v = {a, b};
    bf16x2 r = __builtin_convertvector(v, bf16x2);
    return __builtin_bit_cast(unsigned, r);
}
DI float rbf(float a) { return bflo(pk2(a, 0.f)); }
DI int kperm(int h, int j) { return 8 * (j >> 2) + 4 * h + (j & 3); }

struct Params {
    const u16* x; const u16* wqkv; const u16* wo; const u16* w1; const u16* w2; const u16* c; const u16* wmod;
    u16* out; char* ws;
};

#define TS(i)
#define TSOUT()

typedef __amdgpu_buffer_rsrc_t rsrc_t;
DI u32x4 ldb_sc1(rsrc_t r, unsigned voff, unsigned soff) { return __builtin_bit_cast(u32x4, __builtin_amdgcn_raw_buffer_load_b128(r, voff, soff, 16)); }
DI void stb_sc1(u32x4 v, rsrc_t r, unsigned voff, unsigned soff) { __builtin_amdgcn_raw_buffer_store_b128(v, r, voff + soff, 0, 16); }
DI float ldf_sc1(rsrc_t r, unsigned voff, unsigned soff) { return __builtin_bit_cast(float, __builtin_amdgcn_raw_buffer_load_b32(r, voff, soff, 16)); }
DI void stf_sc1(float v, rsrc_t r, unsigned voff, unsigned soff) { __builtin_amdgcn_raw_buffer_store_b32(__builtin_bit_cast(unsigned, v), r, voff, soff, 16); }
DI f32x2 ldf2_sc1(rsrc_t r, unsigned voff, unsigned soff) { return __builtin_bit_cast(f32x2, __builtin_amdgcn_raw_buffer_load_b64(r, voff, soff, 16)); }
DI void stf2_sc1(f32x2 v, rsrc_t r, unsigned voff, unsigned soff) { __builtin_amdgcn_raw_buffer_store_b64(__builtin_bit_cast(u32x2, v), r, voff, soff, 16); }
DI void drain() { asm volatile("s_waitcnt vmcnt(0)" ::: "memory"); }
DI void set_flag(u64* fl, int idx, int lane, u64 tag) { if (lane == 0) __hip_atomic_store(fl + idx * 16, tag, RLX_AGENT); }
#define SLP 1
DI void wait_flags(u64* fl, int idx, u64 tag) {
    bool pend = idx >= 0;
    const u64* f = fl + (pend ? idx : 0) * 16;
#pragma nounroll
    for (int spins = 0; spins < (1 << 20); spins++) {
        if (pend) pend = __hip_atomic_load(f, RLX_AGENT) != tag;
        if (!__any(pend)) break;
        __builtin_amdgcn_s_sleep(SLP);
    }
    __builtin_amdgcn_fence(__ATOMIC_ACQUIRE, "wavefront");
}
DI u32x2 trd(const char* p) { return __builtin_bit_cast(u32x2, __builtin_amdgcn_ds_read_tr16_b64_v4i16((LAS s16x4*)p)); }

constexpr int R1 = 114688;
#define LDF1(base, f) ldb_sc1(rs, l16 + ((f) & 3) * 1024, (base) + ((f) >> 2) * 4096)

DI void st_tile(char* dst, const f32x16& v) {
#pragma unroll
    for (int q = 0; q < 4; q++) *(f32x4*)(dst + q * 1024) = f32x4{v[4 * q], v[4 * q + 1], v[4 * q + 2], v[4 * q + 3]};
}
DI void add_tile(const char* src, f32x16& v) {
#pragma unroll
    for (int q = 0; q < 4; q++) {
        const f32x4 t = *(const f32x4*)(src + q * 1024);
        v[4 * q] += t[0]; v[4 * q + 1] += t[1]; v[4 * q + 2] += t[2]; v[4 * q + 3] += t[3];
    }
}
DI u32x4 pack8(const u16* v) {
    u32x4 o;
#pragma unroll
    for (int j = 0; j < 4; j++) o[j] = (unsigned)v[2 * j] | ((unsigned)v[2 * j + 1] << 16);
    return o;
}

typedef __attribute__((ext_vector_type(2))) short s16x2;
DI unsigned relu_pk(unsigned v) {
    const s16x2 z = {0, 0};
    return __builtin_bit_cast(unsigned, __builtin_elementwise_max(__builtin_bit_cast(s16x2, v), z));
}
DI f32x2 fma2(f32x2 a, f32x2 b, f32x2 c) { return __builtin_elementwise_fma(a, b, c); }
DI unsigned pk2v(f32x2 v) { return __builtin_bit_cast(unsigned, __builtin_convertvector(v, bf16x2)); }
DI f32x2 up2(unsigned v) { return f32x2{bflo(v), bfhi(v)}; }
DI f32x4 ldf4_sc1(rsrc_t r, unsigned voff, unsigned soff) { return __builtin_bit_cast(f32x4, __builtin_amdgcn_raw_buffer_load_b128(r, voff, soff, 16)); }

DI void xpose2(char* scr, int lane, u32x4& v0, u32x4& v1) {
    *(u32x4*)(scr + (lane >> 3) * 144 + (lane & 7) * 16) = v0;
    *(u32x4*)(scr + (8 + (lane >> 3)) * 144 + (lane & 7) * 16) = v1;
    const char* rp = scr + (4 * (lane >> 5) + ((lane & 15) >> 2)) * 144 + ((lane >> 4) & 1) * 32 + (lane & 3) * 8;
    const u32x2 a0 = trd(rp), a1 = trd(rp + 8 * 144), b0 = trd(rp + 64), b1 = trd(rp + 64 + 8 * 144);
    v0 = u32x4{a0[0], a0[1], a1[0], a1[1]};
    v1 = u32x4{b0[0], b0[1], b1[0], b1[1]};
}

__global__ __launch_bounds__(512, 2) void k_main(Params p) {
    __shared__ __attribute__((aligned(16))) char smem[163840];
    const int b = blockIdx.x, g8 = b & 7, idx = b >> 3;
    const int t = threadIdx.x, lane = t & 63, w = __builtin_amdgcn_readfirstlane(t >> 6), c = lane & 31, h = lane >> 5;
    const int wm = w & 3, wh = w >> 2;
    const rsrc_t rs = __builtin_amdgcn_make_buffer_rsrc(p.ws, 0, WS_END, 0x00020000);
    u64* fl = (u64*)(p.ws + OFF_FLAGS);
    const u64 tag = (llvm_dispatch_id() + 1) * 0x9E3779B97F4A7C15ull ^ (u64)(size_t)__builtin_amdgcn_dispatch_ptr();
    const unsigned l16 = lane * 16;
    const bool gemm_blk = idx < 28;
    int tsv = 0;
    TS(0);
    const int mt2 = 4 * g8 + (gemm_blk ? idx / 7 : 0), ng = gemm_blk ? idx % 7 : 0;
    const int pn1 = 2 * g8 + (gemm_blk ? idx / 14 : 0), cg = gemm_blk ? idx % 14 : 0;
    const int pnl = 2 * g8 + (idx >> 4), head = idx & 15;
    const int mt = pnl * 4 + wm;
    char* sW = smem;
    char* sKV = smem + R1;
    const int i16 = lane & 15;

    if (b < 42) {
        float* sRed = (float*)(smem + 88 * 1024);
        u32x4 mw[7];
#pragma unroll
        for (int i = 0; i < 7; i++) mw[i] = *(const u32x4*)(p.wmod + (size_t)(56 * w + 8 * i + (lane >> 3)) * 2688 + b * 64 + (lane & 7) * 8);
        const float c0 = bf2f(p.c[56 * w + (lane < 56 ? lane : 0)]);
        SB();
        const float s0 = rbf(c0 / (1.f + __expf(-c0)));
        f32x2 acc[4] = {{0.f, 0.f}, {0.f, 0.f}, {0.f, 0.f}, {0.f, 0.f}};
#pragma unroll
        for (int i = 0; i < 7; i++) {
            const float sv = __shfl(s0, 8 * i + (lane >> 3));
#pragma unroll
            for (int j = 0; j < 4; j++) acc[j] = fma2(up2(mw[i][j]), f32x2{sv, sv}, acc[j]);
        }
        *(f32x4*)(sRed + (w * 8 + (lane >> 3)) * 64 + (lane & 7) * 8) = f32x4{acc[0][0], acc[0][1], acc[1][0], acc[1][1]};
        *(f32x4*)(sRed + (w * 8 + (lane >> 3)) * 64 + (lane & 7) * 8 + 4) = f32x4{acc[2][0], acc[2][1], acc[3][0], acc[3][1]};
        float s = 0.f;
#pragma unroll
        for (int i = 0; i < 8; i++) s += sRed[(w * 8 + i) * 64 + lane];
        sRed[4096 + w * 64 + lane] = s;
        __syncthreads();
        if (w == 7) {
            s = 0.f;
#pragma unroll
            for (int i = 0; i < 8; i++) s += sRed[4096 + i * 64 + lane];
            s = rbf(s);
            if ((b >= 7 && b < 14) || (b >= 28 && b < 35)) s = rbf(1.f + s);
            if (b < 14) {
                const u64 gr = ((u64)(__float_as_uint(s) >> 16) << 48) | (tag & 0xffffffffffffull);
                __hip_atomic_store((u64*)(p.ws + OFF_MODG) + b * 64 + lane, gr, RLX_AGENT);
            } else {
                stf_sc1(s, rs, lane * 4, OFF_MODP + b * 256);
                drain();
                set_flag(fl, FL_MOD + b, lane, tag);
            }
            TS(53);
        }
        if (b < 14) __syncthreads();
    }
    float touch0 = 0.f;
    if (w == 0) {
        char* scr = smem + R1;
        const int r = lane & 7, q = lane >> 3;
        const char* xs = (const char*)(p.x + (size_t)(b * 8 + r) * D_) + q * 16;
        u32x4 v[7];
#pragma unroll
        for (int i = 0; i < 7; i++) v[i] = *(const u32x4*)(xs + i * 128);
        SB();
        f32x2 xf[28];
        f32x2 s2 = {0.f, 0.f};
#pragma unroll
        for (int i = 0; i < 7; i++)
#pragma unroll
            for (int j = 0; j < 4; j++) { xf[i * 4 + j] = up2(v[i][j]); s2 += xf[i * 4 + j]; }
        float sum = s2[0] + s2[1];
        sum += __shfl_xor(sum, 8); sum += __shfl_xor(sum, 16); sum += __shfl_xor(sum, 32);
        const float mean = sum * (1.f / D_);
        f32x2 q2 = {0.f, 0.f};
#pragma unroll
        for (int i = 0; i < 28; i++) { xf[i] -= f32x2{mean, mean}; q2 = fma2(xf[i], xf[i], q2); }
        float m2 = q2[0] + q2[1];
        m2 += __shfl_xor(m2, 8); m2 += __shfl_xor(m2, 16); m2 += __shfl_xor(m2, 32);
        const float rstd = rsqrtf(m2 * (1.f / D_) + 1e-5f);
#pragma unroll
        for (int i = 0; i < 28; i++) xf[i] *= f32x2{rstd, rstd};
        TS(45);
        {
            const u64* gp = (const u64*)(p.ws + OFF_MODG) + lane;
            float* sg = (float*)scr;
#pragma nounroll
            for (int spins = 0; spins < (1 << 20); spins++) {
                u64 gv[14];
#pragma unroll
                for (int i = 0; i < 14; i++) gv[i] = __hip_atomic_load(gp + i * 64, RLX_AGENT);
                bool ok = true;
#pragma unroll
                for (int i = 0; i < 14; i++) { ok = ok && ((gv[i] ^ tag) << 16) == 0; sg[i * 64 + lane] = __uint_as_float((unsigned)(gv[i] >> 32) & 0xffff0000u); }
                if (__all(ok)) break;
                __builtin_amdgcn_s_sleep(1);
            }
        }
        TS(46);
        const unsigned dst = OFF_XF + (b >> 2) * KS_D * 1024 + (q >> 1) * 1024 + ((q & 1) * 32 + (b & 3) * 8 + r) * 16;
#pragma unroll
        for (int i = 0; i < 7; i++) {
            const f32x4 g0 = *(const f32x4*)(scr + 1792 + q * 32 + i * 256), g1 = *(const f32x4*)(scr + 1792 + q * 32 + 16 + i * 256);
            const f32x4 s0 = *(const f32x4*)(scr + q * 32 + i * 256), s1 = *(const f32x4*)(scr + q * 32 + 16 + i * 256);
            u32x4 o;
            o[0] = pk2v(fma2(xf[4 * i + 0], f32x2{g0[0], g0[1]}, f32x2{s0[0], s0[1]}));
            o[1] = pk2v(fma2(xf[4 * i + 1], f32x2{g0[2], g0[3]}, f32x2{s0[2], s0[3]}));
            o[2] = pk2v(fma2(xf[4 * i + 2], f32x2{g1[0], g1[1]}, f32x2{s1[0], s1[1]}));
            o[3] = pk2v(fma2(xf[4 * i + 3], f32x2{g1[2], g1[3]}, f32x2{s1[2], s1[3]}));
            stb_sc1(o, rs, dst, i * 4096);
        }
        TS(47);
        drain();
        set_flag(fl, FL_XF + b, lane, tag);
    } else {
        const int rl = lane / 15, pc = lane % 15, part = pc / 5, pp = pc % 5;
        const int odd = head & 1;
        const unsigned col0 = part * 896 + head * 56 - odd * 8 + pp * 16;
        const char* wbase = (const char*)p.wqkv;
        char* dst = sW + (64 * (w - 1) + rl) * 192 + part * 64 + pp * 16 - odd * 8;
        {
            u32x4 v[16];
#pragma unroll
            for (int i = 0; i < 16; i++) {
                unsigned off = (unsigned)(64 * (w - 1) + i * 4 + (rl < 4 ? rl : 0)) * 2688 + col0;
                off = off < 448u * 2688u - 16u ? off : 448u * 2688u - 16u;
                v[i] = *(const u32x4*)(wbase + off);
            }
            if (w >= 6) {
                const unsigned ln = (b * 2 + (w - 6)) * 64 + lane;
                const char* tp = ln < 3136 ? (const char*)p.wo + ln * 128 : (ln < 15680 ? (const char*)p.w1 + (ln - 3136) * 128 : (const char*)p.w2 + (ln < 28224 ? ln - 15680 : 0) * 128);
                touch0 = *(const float*)tp;
            }
            SB();
            if (rl < 4) {
#pragma unroll
                for (int i = 0; i < 16; i++) {
                    char* d = dst + i * 4 * 192;
                    if (odd) {
                        if (pp > 0) *(u32x2*)(d) = u32x2{v[i][0], v[i][1]};
                        if (pp < 4) *(u32x2*)(d + 8) = u32x2{v[i][2], v[i][3]};
                    } else if (pp < 4) *(u32x4*)(d) = v[i];
                }
            }
            SB();
        }
    }
    TS(1);
    f32x16 ak = {0}, av = {0};
    u32x4 wo0[8], wo1[8];
    u32x4 a[14];
    const char* bw = sW + (wh * 14 * 16 + 8 * h + (i16 >> 2)) * 192 + ((lane >> 4) & 1) * 32 + (i16 & 3) * 8;
    {
        const unsigned srcA = OFF_XF + (mt * KS_D + wh * 14) * 1024;
        wait_flags(fl, lane < 4 ? FL_XF + mt * 4 + lane : -1, tag);
        TS(4);
#pragma unroll
        for (int i = 0; i < 8; i++) a[i] = LDF1(srcA, i);
        SB();
        asm volatile("" :: "v"(touch0));
        __syncthreads();
        TS(5);
        u32x2 f[3][4];
#define LDW(i) do { _Pragma("unroll") for (int q = 0; q < 2; q++) { f[(i) % 3][2 * q] = trd(bw + (i) * 3072 + 64 + q * 64); f[(i) % 3][2 * q + 1] = trd(bw + (i) * 3072 + 768 + 64 + q * 64); } } while (0)
        LDW(0); LDW(1);
        SB();
#pragma unroll
        for (int i = 0; i < 14; i++) {
            if (i + 2 < 14) LDW(i + 2);
            if (i + 8 < 14) a[i + 8] = LDF1(srcA, i + 8);
            SB();
            const u32x4 fk = {f[i % 3][0][0], f[i % 3][0][1], f[i % 3][1][0], f[i % 3][1][1]};
            const u32x4 fv = {f[i % 3][2][0], f[i % 3][2][1], f[i % 3][3][0], f[i % 3][3][1]};
            ak = MFMA(fk, a[i], ak);
            av = MFMA(a[i], fv, av);
            SB();
        }
    }
    TS(8);
    if (wh) st_tile(sKV + w * 4096 + l16, ak); else st_tile(sKV + w * 4096 + l16, av);
    __syncthreads();
    {
        u32x4 o0, o1;
        if (wh == 0) {
            add_tile(sKV + (w ^ 4) * 4096 + l16, ak);
#pragma unroll
            for (int j = 0; j < 4; j++) { o0[j] = pk2(ak[2 * j], ak[2 * j + 1]); o1[j] = pk2(ak[8 + 2 * j], ak[8 + 2 * j + 1]); }
            if (h) { o1[2] = 0; o1[3] = 0; }
        } else {
            add_tile(sKV + (w ^ 4) * 4096 + l16, av);
            o0[0] = pk2(av[0], av[1]); o0[1] = pk2(av[2], av[3]); o0[2] = pk2(av[8], av[9]); o0[3] = pk2(av[10], av[11]);
            o1[0] = pk2(av[4], av[5]); o1[1] = pk2(av[6], av[7]); o1[2] = pk2(av[12], av[13]); o1[3] = pk2(av[14], av[15]);
            if (c >= DH) { const unsigned z = c == DH ? 0x3f803f80u : 0u; o0 = u32x4{z, z, z, z}; o1 = o0; }
        }
        const unsigned lo = wh * 2048 + (c >> 4) * 1024 + h * 256 + (c & 15) * 16;
        stb_sc1(o0, rs, lo, OFF_KV + (head * 64 + mt) * 4096);
        stb_sc1(o1, rs, lo + 512, OFF_KV + (head * 64 + mt) * 4096);
        *(u32x4*)(sKV + (8 + wm) * 4096 + lo) = o0;
        *(u32x4*)(sKV + (8 + wm) * 4096 + lo + 512) = o1;
    }
    TS(9);
    __syncthreads();
    u32x4 qf;
    {
        f32x16 aq = {0};
        u32x2 f[3][2];
#define LDQ(i) do { f[(i) % 3][0] = trd(bw + (i) * 3072); f[(i) % 3][1] = trd(bw + (i) * 3072 + 768); } while (0)
        LDQ(0); LDQ(1);
        SB();
#pragma unroll
        for (int i = 0; i < 14; i++) {
            if (i + 2 < 14) LDQ(i + 2);
            SB();
            aq = MFMA((u32x4{f[i % 3][0][0], f[i % 3][0][1], f[i % 3][1][0], f[i % 3][1][1]}), a[i], aq);
            SB();
            if (i == 5) { drain(); set_flag(fl, FL_KV + (head * 64 + mt) * 2 + wh, lane, tag); SB(); }
        }
        char* qx = smem + 88 * 1024;
        if ((c >> 4) != wh) {
#pragma unroll
            for (int q = 0; q < 4; q++) *(f32x4*)(qx + w * 2048 + q * 512 + (h * 16 + (c & 15)) * 16) = f32x4{aq[4 * q], aq[4 * q + 1], aq[4 * q + 2], aq[4 * q + 3]};
        } else {
#pragma unroll
            for (int q = 0; q < 4; q++) *(f32x4*)(sKV + w * 4096 + q * 512 + (h * 16 + (c & 15)) * 16 + 2048) = f32x4{aq[4 * q], aq[4 * q + 1], aq[4 * q + 2], aq[4 * q + 3]};
        }
        __syncthreads();
        const int g4 = lane >> 4, s = g4 >> 1, hq = g4 & 1;
        const char* p0 = qx + (w ^ 4) * 2048 + (2 * s) * 512 + (hq * 16 + i16) * 16;
        const char* p1 = sKV + w * 4096 + 2048 + (2 * s) * 512 + (hq * 16 + i16) * 16;
        const f32x4 x0 = *(const f32x4*)(p0), x1 = *(const f32x4*)(p0 + 512);
        const f32x4 y0 = *(const f32x4*)(p1), y1 = *(const f32x4*)(p1 + 512);
        const float qs = 0.18898223650461362f * 1.4426950408889634f;
        const f32x4 z0 = (x0 + y0) * qs, z1 = (x1 + y1) * qs;
        qf[0] = pk2(z0[0], z0[1]); qf[1] = pk2(z0[2], z0[3]); qf[2] = pk2(z1[0], z1[1]); qf[3] = pk2(z1[2], z1[3]);
        if (g4 == 3) { qf[2] = 0; qf[3] = 0; }
    }
    TS(10);
    u32x4 w1r[14];
    const u16* w1p = p.w1 + (size_t)(56 * w + (lane >> 4)) * DFF + cg * 128 + i16 * 8;
    const int rr = lane >> 3;
    const u16* wop = p.wo + (size_t)(wm * 4 * DH + rr) * D_ + ng * 64 + (lane & 7) * 8;
    {
        const int kt = pnl * 4 - 8 + w;
        wait_flags(fl, lane < 2 ? (kt >= 0 ? FL_KV + (head * 64 + kt) * 2 + lane : -1) : (lane < 5 ? FL_MOD + (lane == 2 ? 14 : lane == 3 ? 28 : 35) + ng : -1), tag);
        TS(11);
        if (kt >= 0) {
            u32x4 kv[4];
#pragma unroll
            for (int i = 0; i < 4; i++) kv[i] = ldb_sc1(rs, l16 + i * 1024, OFF_KV + (head * 64 + kt) * 4096);
#pragma unroll
            for (int i = 0; i < 4; i++) *(u32x4*)(sKV + w * 4096 + i * 1024 + l16) = kv[i];
        } else {
#pragma unroll
            for (int i = 0; i < 4; i++) *(u32x4*)(sKV + w * 4096 + i * 1024 + l16) = u32x4{0, 0, 0, 0};
        }
        __syncthreads();
        TS(12);
        const int T = mt;
        const char* kvb = sKV + wm * 4096 + l16;
        const int g4 = lane >> 4, ql = wh * 16 + i16;
        f32x4 s[18];
        const float NEG = -1e30f;
        u32x4 kf[3][2];
        kf[0][0] = *(const u32x4*)(kvb); kf[0][1] = *(const u32x4*)(kvb + 1024);
        kf[1][0] = *(const u32x4*)(kvb + 4096); kf[1][1] = *(const u32x4*)(kvb + 4096 + 1024);
        f32x4 mlo[2], mhi[2];
#pragma unroll
        for (int u = 0; u < 2; u++)
#pragma unroll
            for (int r = 0; r < 4; r++) {
                const bool gt = (16 * u + 4 * g4 + r) > ql;
                mlo[u][r] = gt ? 0.f : NEG; mhi[u][r] = gt ? NEG : 0.f;
            }
        SB();
#pragma unroll
        for (int i = 0; i < 9; i++) {
            if (i + 2 < 9) { kf[(i + 2) % 3][0] = *(const u32x4*)(kvb + (i + 2) * 4096); kf[(i + 2) % 3][1] = *(const u32x4*)(kvb + (i + 2) * 4096 + 1024); }
            if (i < 8) {
                const int hd = i >> 1, sx = i & 1;
                wo0[i] = *(const u32x4*)(wop + (size_t)(hd * DH + 8 * sx) * D_);
                wo1[i] = *(const u32x4*)(wop + (size_t)(hd * DH + 8 * sx + ((sx == 1 && rr >= 4) ? 0 : 16)) * D_);
            }
            SB();
            const f32x4 z = {0, 0, 0, 0};
            s[2 * i] = MFMA16(kf[i % 3][0], qf, i == 0 ? mlo[0] : (i == 8 ? mhi[0] : z));
            s[2 * i + 1] = MFMA16(kf[i % 3][1], qf, i == 0 ? mlo[1] : (i == 8 ? mhi[1] : z));
            SB();
        }
        u32x4 vf[2][2];
        vf[0][0] = *(const u32x4*)(kvb + 2048); vf[0][1] = *(const u32x4*)(kvb + 3072);
        SB();
        if (__builtin_expect(T < 8, 0)) {
#pragma unroll
            for (int i = 0; i < 8; i++)
                if (T - 8 + i < 0) { s[2 * i] = f32x4{NEG, NEG, NEG, NEG}; s[2 * i + 1] = f32x4{NEG, NEG, NEG, NEG}; asm volatile("" : "+v"(s[2 * i]), "+v"(s[2 * i + 1])); }
        }
        float m0 = fmaxf(s[0][0], s[0][1]), m1 = fmaxf(s[0][2], s[0][3]);
#pragma unroll
        for (int i = 1; i < 18; i++) { m0 = fmaxf(fmaxf(m0, s[i][0]), s[i][1]); m1 = fmaxf(fmaxf(m1, s[i][2]), s[i][3]); }
        float m = fmaxf(m0, m1);
        m = fmaxf(m, __shfl_xor(m, 16));
        m = fmaxf(m, __shfl_xor(m, 32));
        f32x4 o0 = {0, 0, 0, 0}, o1 = {0, 0, 0, 0};
        SB();
#pragma unroll
        for (int i = 0; i < 9; i++) {
            if (i + 1 < 9) { vf[(i + 1) & 1][0] = *(const u32x4*)(kvb + (i + 1) * 4096 + 2048); vf[(i + 1) & 1][1] = *(const u32x4*)(kvb + (i + 1) * 4096 + 3072); }
            if (i < 7) { w1r[2 * i] = *(const u32x4*)(w1p + (size_t)(8 * i) * DFF); w1r[2 * i + 1] = *(const u32x4*)(w1p + (size_t)(8 * i + 4) * DFF); }
            u32x4 pf;
            pf[0] = pk2(__builtin_amdgcn_exp2f(s[2 * i][0] - m), __builtin_amdgcn_exp2f(s[2 * i][1] - m));
            pf[1] = pk2(__builtin_amdgcn_exp2f(s[2 * i][2] - m), __builtin_amdgcn_exp2f(s[2 * i][3] - m));
            pf[2] = pk2(__builtin_amdgcn_exp2f(s[2 * i + 1][0] - m), __builtin_amdgcn_exp2f(s[2 * i + 1][1] - m));
            pf[3] = pk2(__builtin_amdgcn_exp2f(s[2 * i + 1][2] - m), __builtin_amdgcn_exp2f(s[2 * i + 1][3] - m));
            SB();
            o0 = MFMA16(vf[i & 1][0], pf, o0);
            o1 = MFMA16(vf[i & 1][1], pf, o1);
            SB();
        }
        const float l = __shfl(o1[0], 48 + i16);
        const float inv = __builtin_amdgcn_rcpf(l);
        o0 *= inv; o1 *= inv;
        u32x4 ov;
        ov[0] = pk2(o0[0], o0[1]); ov[1] = pk2(o0[2], o0[3]); ov[2] = pk2(o1[0], o1[1]); ov[3] = pk2(o1[2], o1[3]);
        stb_sc1(ov, rs, (g4 >> 1) * 1024 + ((g4 & 1) * 32 + ql) * 16, OFF_O + (T * KS_O + head * 2) * 1024);
        TS(13);
        drain();
        set_flag(fl, FL_O + T * 32 + head * 2 + wh, lane, tag);
    }
    TS(14);
    if (!gemm_blk) {
        const int gb = (idx - 28) * 8 + g8;
        if (gb < 28) {
            float* sRed = (float*)smem;
            u32x4 mw[7];
#pragma unroll
            for (int i = 0; i < 7; i++) mw[i] = *(const u32x4*)(p.w1 + (size_t)(56 * w + 8 * i + (lane >> 3)) * DFF + gb * 64 + (lane & 7) * 8);
            wait_flags(fl, lane < 14 ? FL_MOD + 21 + lane : -1, tag);
            const unsigned kk = (56 * w + (lane < 56 ? lane : 0)) * 4;
            const float gk = ldf_sc1(rs, kk, OFF_MODP + 1792 * 4), sk = ldf_sc1(rs, kk, OFF_MODP + 1344 * 4);
            f32x2 ag[4] = {{0.f, 0.f}, {0.f, 0.f}, {0.f, 0.f}, {0.f, 0.f}}, ab[4] = {{0.f, 0.f}, {0.f, 0.f}, {0.f, 0.f}, {0.f, 0.f}};
#pragma unroll
            for (int i = 0; i < 7; i++) {
                const float gv = __shfl(gk, 8 * i + (lane >> 3)), sv = __shfl(sk, 8 * i + (lane >> 3));
#pragma unroll
                for (int j = 0; j < 4; j++) { const f32x2 e = up2(mw[i][j]); ag[j] = fma2(e, f32x2{gv, gv}, ag[j]); ab[j] = fma2(e, f32x2{sv, sv}, ab[j]); }
            }
            __syncthreads();
            float* pr = sRed + (w * 8 + (lane >> 3)) * 128 + (lane & 7) * 8;
            *(f32x4*)(pr) = f32x4{ag[0][0], ag[0][1], ag[1][0], ag[1][1]}; *(f32x4*)(pr + 4) = f32x4{ag[2][0], ag[2][1], ag[3][0], ag[3][1]};
            *(f32x4*)(pr + 64) = f32x4{ab[0][0], ab[0][1], ab[1][0], ab[1][1]}; *(f32x4*)(pr + 68) = f32x4{ab[2][0], ab[2][1], ab[3][0], ab[3][1]};
            float s0 = 0.f, s1 = 0.f;
#pragma unroll
            for (int i = 0; i < 8; i++) { s0 += sRed[(w * 8 + i) * 128 + lane]; s1 += sRed[(w * 8 + i) * 128 + 64 + lane]; }
            sRed[8192 + w * 128 + lane] = s0; sRed[8192 + w * 128 + 64 + lane] = s1;
            __syncthreads();
            if (w == 0) {
                s0 = 0.f; s1 = 0.f;
#pragma unroll
                for (int i = 0; i < 8; i++) { s0 += sRed[8192 + i * 128 + lane]; s1 += sRed[8192 + i * 128 + 64 + lane]; }
                stf_sc1(s0, rs, lane * 4, OFF_GB2 + gb * 512);
                stf_sc1(s1, rs, lane * 4 + 256, OFF_GB2 + gb * 512);
                drain();
                set_flag(fl, FL_GB2 + gb, lane, tag);
            }
        }
        TSOUT();
        return;
    }

    const int umt = 2 * mt2 + wh, unt = 2 * ng + (wm >> 1), us = wm & 1;
    u32x2 xr[2];
    f32x4 gav[2], gbv[2], g2v[2];
    u32x4 w2a[14], w2b[14];
    const u16* w2p = p.w2 + (size_t)(224 * w + (lane >> 3)) * D_ + ng * 64 + (lane & 7) * 8;
#define LDW2(i) do { w2a[i] = *(const u32x4*)(w2p + (size_t)(16 * (i)) * D_); w2b[i] = *(const u32x4*)(w2p + (size_t)(16 * (i) + 8) * D_); } while (0)
    {
        const u16* xrow = p.x + (size_t)(umt * 32 + c) * D_ + unt * 32 + 16 * us + 4 * h;
        xr[0] = *(const u32x2*)(xrow); xr[1] = *(const u32x2*)(xrow + 8);
        const unsigned no = (unt * 32 + 16 * us) * 4;
        gav[0] = ldf4_sc1(rs, h * 16, OFF_MODP + 896 * 4 + no); gav[1] = ldf4_sc1(rs, h * 16 + 32, OFF_MODP + 896 * 4 + no);
        gbv[0] = ldf4_sc1(rs, h * 16, OFF_MODP + 2240 * 4 + no); gbv[1] = ldf4_sc1(rs, h * 16 + 32, OFF_MODP + 2240 * 4 + no);
        g2v[0] = ldf4_sc1(rs, h * 16, OFF_MODP + 1792 * 4 + no); g2v[1] = ldf4_sc1(rs, h * 16 + 32, OFF_MODP + 1792 * 4 + no);
        LDW2(0);
        SB();
        char* scr = smem + 40960 + w * 4096;
#pragma unroll
        for (int k = 0; k < 8; k++) {
            xpose2(scr, lane, wo0[k], wo1[k]);
            if ((k & 1) && h) { wo0[k][2] = 0; wo0[k][3] = 0; wo1[k][2] = 0; wo1[k][3] = 0; }
        }
    }
    TS(15);
    wait_flags(fl, lane < 8 ? FL_O + umt * 32 + 8 * wm + lane : -1, tag);
    TS(16);
    u32x4 x1p;
    {
        u32x4 a0[8];
#pragma unroll
        for (int i = 0; i < 8; i++) a0[i] = LDF1(OFF_O + (umt * KS_O + wm * 8) * 1024, i);
        SB();
        f32x16 c0 = {0}, c1 = {0};
#pragma unroll
        for (int i = 0; i < 8; i++) {
            c0 = MFMA(wo0[i], a0[i], c0);
            c1 = MFMA(wo1[i], a0[i], c1);
            SB();
            if (i == 0) LDW2(1);
            if (i == 2) LDW2(2);
            if (i == 4) LDW2(3);
            if (i == 6) LDW2(4);
            SB();
        }
        TS(17);
        __syncthreads();
        char* red = smem + R1 + wh * 24576;
        f32x4 r0, r1;
#define WR_HU(hu, cc, o) do { const int slot = (hu) * 3 + (wm > (hu) ? wm - 1 : wm); \
            *(f32x4*)(red + slot * 2048 + l16) = f32x4{cc[o], cc[o + 1], cc[o + 2], cc[o + 3]}; \
            *(f32x4*)(red + slot * 2048 + 1024 + l16) = f32x4{cc[o + 4], cc[o + 5], cc[o + 6], cc[o + 7]}; } while (0)
#define KP_HU(cc, o) do { r0 = f32x4{cc[o], cc[o + 1], cc[o + 2], cc[o + 3]}; r1 = f32x4{cc[o + 4], cc[o + 5], cc[o + 6], cc[o + 7]}; } while (0)
        if (wm == 0) { WR_HU(1, c0, 8); WR_HU(2, c1, 0); WR_HU(3, c1, 8); KP_HU(c0, 0); }
        else if (wm == 1) { WR_HU(0, c0, 0); WR_HU(2, c1, 0); WR_HU(3, c1, 8); KP_HU(c0, 8); }
        else if (wm == 2) { WR_HU(0, c0, 0); WR_HU(1, c0, 8); WR_HU(3, c1, 8); KP_HU(c1, 0); }
        else { WR_HU(0, c0, 0); WR_HU(1, c0, 8); WR_HU(2, c1, 0); KP_HU(c1, 8); }
        __syncthreads();
#pragma unroll
        for (int s3 = 0; s3 < 3; s3++) {
            r0 += *(const f32x4*)(red + (wm * 3 + s3) * 2048 + l16);
            r1 += *(const f32x4*)(red + (wm * 3 + s3) * 2048 + 1024 + l16);
        }
        TS(18);
        x1p[0] = pk2v(fma2(f32x2{gav[0][0], gav[0][1]}, f32x2{r0[0], r0[1]}, up2(xr[0][0])));
        x1p[1] = pk2v(fma2(f32x2{gav[0][2], gav[0][3]}, f32x2{r0[2], r0[3]}, up2(xr[0][1])));
        x1p[2] = pk2v(fma2(f32x2{gav[1][0], gav[1][1]}, f32x2{r1[0], r1[1]}, up2(xr[1][0])));
        x1p[3] = pk2v(fma2(f32x2{gav[1][2], gav[1][3]}, f32x2{r1[2], r1[3]}, up2(xr[1][1])));
        f32x2 xv[4];
#pragma unroll
        for (int j = 0; j < 4; j++) xv[j] = up2(x1p[j]);
        u32x4 xg;
        xg[0] = pk2v(xv[0] * f32x2{g2v[0][0], g2v[0][1]}); xg[1] = pk2v(xv[1] * f32x2{g2v[0][2], g2v[0][3]});
        xg[2] = pk2v(xv[2] * f32x2{g2v[1][0], g2v[1][1]}); xg[3] = pk2v(xv[3] * f32x2{g2v[1][2], g2v[1][3]});
        stb_sc1(xg, rs, l16, OFF_X1 + (umt * KS_D + 2 * unt + us) * 1024);
        const f32x2 s2 = (xv[0] + xv[1]) + (xv[2] + xv[3]);
        float sum = s2[0] + s2[1];
        sum += __shfl_xor(sum, 32);
        const float mean = sum * (1.f / 16.f);
        f32x2 q2 = {0.f, 0.f};
#pragma unroll
        for (int j = 0; j < 4; j++) { const f32x2 d = xv[j] - f32x2{mean, mean}; q2 = fma2(d, d, q2); }
        float m2 = q2[0] + q2[1];
        m2 += __shfl_xor(m2, 32);
        if (h == 0) *(f32x2*)(red + wm * 6144 + c * 8) = f32x2{mean, m2};
        drain();
        set_flag(fl, FL_X1 + umt * 32 + 2 * unt + us, lane, tag);
        SB();
    }
    TS(19);
    char* scr2 = smem + R1 + w * 6144 + 1024;
    LDW2(5); LDW2(6); LDW2(7); LDW2(8);
    SB();
    {
        char* d = smem + (56 * w + (lane >> 4)) * 256 + ((i16 ^ ((lane >> 4) << 2)) * 16);
#pragma unroll
        for (int i = 0; i < 14; i++) *(u32x4*)(d + i * 1024) = w1r[i];
    }
    __syncthreads();
    float* sGB = (float*)(smem + R1 + wh * 24576 + 6144 + 4096);
    if (wm == 0) {
        const f32x2* sp = (const f32x2*)(smem + R1 + wh * 24576) + c;
        const f32x2 a0 = sp[0], a1 = sp[768], a2 = sp[1536], a3 = sp[2304];
        const float mu = 0.25f * ((a0[0] + a1[0]) + (a2[0] + a3[0]));
        const float d0 = a0[0] - mu, d1 = a1[0] - mu, d2 = a2[0] - mu, d3 = a3[0] - mu;
        const float m2 = (a0[1] + a1[1]) + (a2[1] + a3[1]) + 16.f * ((d0 * d0 + d1 * d1) + (d2 * d2 + d3 * d3));
        if (h == 0) stf2_sc1(f32x2{mu, m2}, rs, c * 8, OFF_ST2 + (umt * 8 + ng) * 256);
        drain();
        set_flag(fl, FL_ST + umt * 8 + ng, lane, tag);
    } else if (wm == 1) {
        wait_flags(fl, lane == 0 ? FL_GB2 + cg * 2 + wh : -1, tag);
        const float g = ldf_sc1(rs, lane * 4, OFF_GB2 + (cg * 2 + wh) * 512), bb = ldf_sc1(rs, lane * 4 + 256, OFF_GB2 + (cg * 2 + wh) * 512);
        sGB[lane] = g; sGB[64 + lane] = bb;
    }
    #pragma unroll
    for (int i = 0; i < 5; i++) xpose2(scr2, lane, w2a[i], w2b[i]);
    const int m1 = pn1 * 4 + wm;
    wait_flags(fl, lane < 28 ? FL_X1 + m1 * 32 + lane : -1, tag);
    TS(20);
    {
        constexpr int PX = 6;
        u32x4 xa[KS_D];
        const unsigned xs = OFF_X1 + m1 * KS_D * 1024;
#pragma unroll
        for (int i = 0; i < PX; i++) xa[i] = LDF1(xs, i);
        SB();
        f32x16 c0 = {0}, c1 = {0};
        u32x2 fb[3][4];
        const int q4 = i16 >> 2;
        const char* bl0 = smem + (4 * h + q4) * 256 + (((8 * wh + 2 * ((lane >> 4) & 1) + ((i16 & 3) >> 1)) ^ (q4 << 2)) * 16) + (i16 & 1) * 8;
        const char* bl1 = smem + (4 * h + q4) * 256 + (((8 * wh + 4 + 2 * ((lane >> 4) & 1) + ((i16 & 3) >> 1)) ^ (q4 << 2)) * 16) + (i16 & 1) * 8;
        const char* bh0 = bl0 + 57344; const char* bh1 = bl1 + 57344;
#define LDFB(S) do { if ((S) < KS_D) { const char* p0_ = (S) < 14 ? bl0 + (S) * 4096 : bh0 + ((S) - 14) * 4096; const char* p1_ = (S) < 14 ? bl1 + (S) * 4096 : bh1 + ((S) - 14) * 4096; \
        fb[(S) % 3][0] = trd(p0_); fb[(S) % 3][1] = trd(p0_ + 2048); fb[(S) % 3][2] = trd(p1_); fb[(S) % 3][3] = trd(p1_ + 2048); } } while (0)
        LDFB(0); LDFB(1);
        SB();
        f32x2 st2[4];
        u64 stf = 0;
        __syncthreads();
#pragma unroll
        for (int S = 0; S < KS_D; S++) {
            LDFB(S + 2);
            SB();
            c0 = MFMA((u32x4{fb[S % 3][0][0], fb[S % 3][0][1], fb[S % 3][1][0], fb[S % 3][1][1]}), xa[S], c0);
            SB();
            if (S + PX < KS_D) xa[S + PX] = LDF1(xs, S + PX);
            if (S == 14) stf = __hip_atomic_load(fl + (FL_ST + m1 * 8 + (lane < 7 ? lane : 0)) * 16, RLX_AGENT);
            if (S == 22) {
                if (!__all(stf == tag)) wait_flags(fl, lane < 7 ? FL_ST + m1 * 8 + lane : -1, tag);
#pragma unroll
                for (int i = 0; i < 4; i++) st2[i] = ldf2_sc1(rs, (h * 4 + i < 7 ? h * 4 + i : 6) * 256 + c * 8, OFF_ST2 + m1 * 8 * 256);
            }
            if (S >= 23) { LDW2(S - 14); }
            SB();
            c1 = MFMA((u32x4{fb[S % 3][2][0], fb[S % 3][2][1], fb[S % 3][3][0], fb[S % 3][3][1]}), xa[S], c1);
            SB();
        }
        TS(21);
        float rsd, nmr;
        {
            const float wl = h ? 0.f : 1.f;
            float s = (st2[0][0] + st2[1][0]) + (st2[2][0] + wl * st2[3][0]);
            s += __shfl_xor(s, 32);
            const float mu = s * (1.f / 7.f);
            const float d0 = st2[0][0] - mu, d1 = st2[1][0] - mu, d2 = st2[2][0] - mu, d3 = st2[3][0] - mu;
            float m2 = (st2[0][1] + st2[1][1]) + (st2[2][1] + wl * st2[3][1]) + 64.f * ((d0 * d0 + d1 * d1) + (d2 * d2 + wl * d3 * d3));
            m2 += __shfl_xor(m2, 32);
            rsd = rsqrtf(m2 * (1.f / D_) + 1e-5f);
            nmr = -mu * rsd;
        }
        const f32x2 rs2 = {rsd, rsd}, nm2 = {nmr, nmr};
#define EPI_Q(e, n, a4, dst) do { \
        const f32x4 G = *(const f32x4*)(sGB + (n) * 32 + 8 * (a4) + 4 * h), B = *(const f32x4*)(sGB + 64 + (n) * 32 + 8 * (a4) + 4 * h); \
        dst[((a4) & 1) * 2] = relu_pk(pk2v(fma2(rs2, f32x2{e[4 * (a4)], e[4 * (a4) + 1]}, fma2(nm2, f32x2{G[0], G[1]}, f32x2{B[0], B[1]})))); \
        dst[((a4) & 1) * 2 + 1] = relu_pk(pk2v(fma2(rs2, f32x2{e[4 * (a4) + 2], e[4 * (a4) + 3]}, fma2(nm2, f32x2{G[2], G[3]}, f32x2{B[2], B[3]})))); } while (0)
        u32x4 eo[4];
#pragma unroll
        for (int a4 = 0; a4 < 4; a4++) { EPI_Q(c0, 0, a4, eo[a4 >> 1]); }
#pragma unroll
        for (int a4 = 0; a4 < 4; a4++) { EPI_Q(c1, 1, a4, eo[2 + (a4 >> 1)]); }
#pragma unroll
        for (int i = 0; i < 4; i++) stb_sc1(eo[i], rs, l16, OFF_H1 + (m1 * KS_F + (cg * 4 + 2 * wh) * 2 + i) * 1024);
        TS(22);
        drain();
        set_flag(fl, FL_H1 + m1 * 32 + cg * 2 + wh, lane, tag);
        SB();
#pragma unroll
        for (int i = 5; i < 14; i++) xpose2(scr2, lane, w2a[i], w2b[i]);
        __syncthreads();
    }
    TS(23);
    {
        const int f0 = (7 * w) >> 1, f1 = (7 * w + 6) >> 1;
        const int fi = f0 + (lane & 3);
        wait_flags(fl, (lane < 8 && fi <= f1) ? FL_H1 + (2 * mt2 + (lane >> 2)) * 32 + fi : -1, tag);
    }
    TS(24);
    f32x16 acc[4];
    {
        constexpr int PD = 5;
        u32x4 a0[14], a1[14];
        const unsigned s0 = OFF_H1 + ((2 * mt2) * KS_F + w * 14) * 1024, s1 = s0 + KS_F * 1024;
#define LDH(i) do { a0[i] = ldb_sc1(rs, l16 + ((i) & 1) * 1024, s0 + ((i) >> 1) * 2048); a1[i] = ldb_sc1(rs, l16 + ((i) & 1) * 1024, s1 + ((i) >> 1) * 2048); } while (0)
#pragma unroll
        for (int i = 0; i < PD; i++) LDH(i);
        SB();
#pragma unroll
        for (int u = 0; u < 4; u++) acc[u] = f32x16{0};
#pragma unroll
        for (int i = 0; i < 14; i++) {
            acc[0] = MFMA(w2a[i], a0[i], acc[0]);
            acc[1] = MFMA(w2b[i], a0[i], acc[1]);
            SB();
            if (i + PD < 14) LDH(i + PD);
            SB();
            acc[2] = MFMA(w2a[i], a1[i], acc[2]);
            acc[3] = MFMA(w2b[i], a1[i], acc[3]);
            SB();
        }
    }
    TS(25);
    {
        char* red = smem + w * 2048 + l16;
#pragma unroll
        for (int hu = 0; hu < 8; hu++) {
            const f32x16& cc = acc[hu >> 1];
            const int r0 = (hu & 1) * 8;
            *(f32x4*)(red + hu * 16384) = f32x4{cc[r0], cc[r0 + 1], cc[r0 + 2], cc[r0 + 3]};
            *(f32x4*)(red + hu * 16384 + 1024) = f32x4{cc[r0 + 4], cc[r0 + 5], cc[r0 + 6], cc[r0 + 7]};
        }
        __syncthreads();
        const char* rd = smem + w * 16384 + l16;
        f32x4 r0 = *(const f32x4*)(rd), r1 = *(const f32x4*)(rd + 1024);
#pragma unroll
        for (int s7 = 1; s7 < 8; s7++) { r0 += *(const f32x4*)(rd + s7 * 2048); r1 += *(const f32x4*)(rd + s7 * 2048 + 1024); }
        TS(26);
        u16* orow = p.out + (size_t)(umt * 32 + c) * D_ + unt * 32 + 16 * us + 4 * h;
        u32x2 o0, o1;
        o0[0] = pk2v(fma2(f32x2{gbv[0][0], gbv[0][1]}, f32x2{r0[0], r0[1]}, up2(x1p[0])));
        o0[1] = pk2v(fma2(f32x2{gbv[0][2], gbv[0][3]}, f32x2{r0[2], r0[3]}, up2(x1p[1])));
        o1[0] = pk2v(fma2(f32x2{gbv[1][0], gbv[1][1]}, f32x2{r1[0], r1[1]}, up2(x1p[2])));
        o1[1] = pk2v(fma2(f32x2{gbv[1][2], gbv[1][3]}, f32x2{r1[2], r1[3]}, up2(x1p[3])));
        *(u32x2*)(orow) = o0;
        *(u32x2*)(orow + 8) = o1;
    }
    TS(27); TSOUT();
}

extern "C" void kernel_launch(void* const* d_in, const int* in_sizes, int n_in,
                              void* d_out, int out_size, void* d_ws, size_t ws_size,
                              hipStream_t stream) {
    Params p;
    p.x = (const u16*)d_in[0]; p.wqkv = (const u16*)d_in[1]; p.wo = (const u16*)d_in[2];
    p.w1 = (const u16*)d_in[4]; p.w2 = (const u16*)d_in[5]; p.c = (const u16*)d_in[6]; p.wmod = (const u16*)d_in[7];
    p.out = (u16*)d_out; p.ws = (char*)d_ws;
    k_main<<<256, 512, 0, stream>>>(p);
}
```

```cpp
#include <hip/hip_runtime.h>
#include <stdint.h>

typedef __attribute__((ext_vector_type(8))) __bf16 bf16x8;
typedef __attribute__((ext_vector_type(2))) __bf16 bf16x2;
typedef __attribute__((ext_vector_type(2))) float f32x2;
typedef __attribute__((ext_vector_type(4))) float f32x4;
typedef __attribute__((ext_vector_type(16))) float f32x16;
typedef __attribute__((ext_vector_type(4))) unsigned u32x4;
typedef __attribute__((ext_vector_type(2))) unsigned u32x2;
typedef __attribute__((ext_vector_type(4))) short s16x4;
typedef unsigned short u16;
typedef unsigned long long u64;

#define DI __device__ __forceinline__
#define MFMA(a, b, c) __builtin_amdgcn_mfma_f32_32x32x16_bf16(__builtin_bit_cast(bf16x8, (a)), __builtin_bit_cast(bf16x8, (b)), (c), 0, 0, 0)
#define MFMA16(a, b, c) __builtin_amdgcn_mfma_f32_16x16x32_bf16(__builtin_bit_cast(bf16x8, (a)), __builtin_bit_cast(bf16x8, (b)), (c), 0, 0, 0)
#define SB() __builtin_amdgcn_sched_barrier(0)
#define RLX_AGENT __ATOMIC_RELAXED, __HIP_MEMORY_SCOPE_AGENT
#define LAS __attribute__((address_space(3)))

extern "C" __device__ unsigned long long llvm_dispatch_id() __asm("llvm.amdgcn.dispatch.id");

constexpr int D_ = 448, DFF = 1792, DH = 28;
constexpr int KS_D = 28, KS_O = 32, KS_F = 112;

constexpr unsigned OFF_FLAGS = 0;
constexpr unsigned OFF_MODP = 1245184;
constexpr unsigned OFF_MODG = OFF_MODP + 16384;
constexpr unsigned OFF_GB2 = OFF_MODG + 16384;
constexpr unsigned OFF_XF = OFF_GB2 + 16384;
constexpr unsigned OFF_KV = OFF_XF + 64 * 28 * 1024;
constexpr unsigned OFF_O = OFF_KV + 16 * 64 * 4096;
constexpr unsigned OFF_X1 = OFF_O + 64 * 32 * 1024;
constexpr unsigned OFF_ST2 = OFF_X1 + 64 * 28 * 1024;
constexpr unsigned OFF_H1 = OFF_ST2 + 2048 * 28 * 8;
constexpr unsigned WS_END = OFF_H1 + 64 * 112 * 1024;

constexpr int FL_MOD = 0;
constexpr int FL_GB2 = 64;
constexpr int FL_XF = 128;
constexpr int FL_KV = 384;
constexpr int FL_O = 2432;
constexpr int FL_X1 = 4480;
constexpr int FL_H1 = 6528;
constexpr int FL_ST = 8576;

DI float bf2f(u16 v) { return __uint_as_float(((unsigned)v) << 16); }
DI float bflo(unsigned v) { return __uint_as_float(v << 16); }
DI float bfhi(unsigned v) { return __uint_as_float(v & 0xffff0000u); }
DI unsigned pk2(float a, float b) {
    f32x2 v = {a, b};
    bf16x2 r = __builtin_convertvector(v, bf16x2);
    return __builtin_bit_cast(unsigned, r);
}
DI float rbf(float a) { return bflo(pk2(a, 0.f)); }
DI int kperm(int h, int j) { return 8 * (j >> 2) + 4 * h + (j & 3); }

struct Params {
    const u16* x; const u16* wqkv; const u16* wo; const u16* w1; const u16* w2; const u16* c; const u16* wmod;
    u16* out; char* ws;
};


typedef __amdgpu_buffer_rsrc_t rsrc_t;
DI u32x4 ldb_sc1(rsrc_t r, unsigned voff, unsigned soff) { return __builtin_bit_cast(u32x4, __builtin_amdgcn_raw_buffer_load_b128(r, voff, soff, 16)); }
DI void stb_sc1(u32x4 v, rsrc_t r, unsigned voff, unsigned soff) { __builtin_amdgcn_raw_buffer_store_b128(v, r, voff + soff, 0, 16); }
DI float ldf_sc1(rsrc_t r, unsigned voff, unsigned soff) { return __builtin_bit_cast(float, __builtin_amdgcn_raw_buffer_load_b32(r, voff, soff, 16)); }
DI void stf_sc1(float v, rsrc_t r, unsigned voff, unsigned soff) { __builtin_amdgcn_raw_buffer_store_b32(__builtin_bit_cast(unsigned, v), r, voff, soff, 16); }
DI f32x2 ldf2_sc1(rsrc_t r, unsigned voff, unsigned soff) { return __builtin_bit_cast(f32x2, __builtin_amdgcn_raw_buffer_load_b64(r, voff, soff, 16)); }
DI void stf2_sc1(f32x2 v, rsrc_t r, unsigned voff, unsigned soff) { __builtin_amdgcn_raw_buffer_store_b64(__builtin_bit_cast(u32x2, v), r, voff, soff, 16); }
DI void drain() { asm volatile("s_waitcnt vmcnt(0)" ::: "memory"); }
DI void set_flag(u64* fl, int idx, int lane, u64 tag) { if (lane == 0) __hip_atomic_store(fl + idx * 16, tag, RLX_AGENT); }
DI void wait_flags(u64* fl, int idx, u64 tag) {
    bool pend = idx >= 0;
    const u64* f = fl + (pend ? idx : 0) * 16;
#pragma nounroll
    for (int spins = 0; spins < (1 << 20); spins++) {
        if (pend) pend = __hip_atomic_load(f, RLX_AGENT) != tag;
        if (!__any(pend)) break;
        __builtin_amdgcn_s_sleep(1);
    }
    __builtin_amdgcn_fence(__ATOMIC_ACQUIRE, "wavefront");
}
DI u32x2 trd(const char* p) { return __builtin_bit_cast(u32x2, __builtin_amdgcn_ds_read_tr16_b64_v4i16((LAS s16x4*)p)); }

constexpr int R1 = 114688;
#define LDF1(base, f) ldb_sc1(rs, l16 + ((f) & 3) * 1024, (base) + ((f) >> 2) * 4096)

DI void st_tile(char* dst, const f32x16& v) {
#pragma unroll
    for (int q = 0; q < 4; q++) *(f32x4*)(dst + q * 1024) = f32x4{v[4 * q], v[4 * q + 1], v[4 * q + 2], v[4 * q + 3]};
}
DI void add_tile(const char* src, f32x16& v) {
#pragma unroll
    for (int q = 0; q < 4; q++) {
        const f32x4 t = *(const f32x4*)(src + q * 1024);
        v[4 * q] += t[0]; v[4 * q + 1] += t[1]; v[4 * q + 2] += t[2]; v[4 * q + 3] += t[3];
    }
}
DI u32x4 pack8(const u16* v) {
    u32x4 o;
#pragma unroll
    for (int j = 0; j < 4; j++) o[j] = (unsigned)v[2 * j] | ((unsigned)v[2 * j + 1] << 16);
    return o;
}

typedef __attribute__((ext_vector_type(2))) short s16x2;
DI unsigned relu_pk(unsigned v) {
    const s16x2 z = {0, 0};
    return __builtin_bit_cast(unsigned, __builtin_elementwise_max(__builtin_bit_cast(s16x2, v), z));
}
DI f32x2 fma2(f32x2 a, f32x2 b, f32x2 c) { return __builtin_elementwise_fma(a, b, c); }
DI unsigned pk2v(f32x2 v) { return __builtin_bit_cast(unsigned, __builtin_convertvector(v, bf16x2)); }
DI f32x2 up2(unsigned v) { return f32x2{bflo(v), bfhi(v)}; }
DI f32x4 ldf4_sc1(rsrc_t r, unsigned voff, unsigned soff) { return __builtin_bit_cast(f32x4, __builtin_amdgcn_raw_buffer_load_b128(r, voff, soff, 16)); }

DI void xpose2(char* scr, int lane, u32x4& v0, u32x4& v1) {
    *(u32x4*)(scr + (lane >> 3) * 144 + (lane & 7) * 16) = v0;
    *(u32x4*)(scr + (8 + (lane >> 3)) * 144 + (lane & 7) * 16) = v1;
    const char* rp = scr + (4 * (lane >> 5) + ((lane & 15) >> 2)) * 144 + ((lane >> 4) & 1) * 32 + (lane & 3) * 8;
    const u32x2 a0 = trd(rp), a1 = trd(rp + 8 * 144), b0 = trd(rp + 64), b1 = trd(rp + 64 + 8 * 144);
    v0 = u32x4{a0[0], a0[1], a1[0], a1[1]};
    v1 = u32x4{b0[0], b0[1], b1[0], b1[1]};
}

__global__ __launch_bounds__(512, 2) void k_main(Params p) {
    __shared__ __attribute__((aligned(16))) char smem[163840];
    const int b = blockIdx.x, g8 = b & 7, idx = b >> 3;
    const int t = threadIdx.x, lane = t & 63, w = __builtin_amdgcn_readfirstlane(t >> 6), c = lane & 31, h = lane >> 5;
    const int wm = w & 3, wh = w >> 2;
    const rsrc_t rs = __builtin_amdgcn_make_buffer_rsrc(p.ws, 0, WS_END, 0x00020000);
    u64* fl = (u64*)(p.ws + OFF_FLAGS);
    const u64 tag = (llvm_dispatch_id() + 1) * 0x9E3779B97F4A7C15ull ^ (u64)(size_t)__builtin_amdgcn_dispatch_ptr();
    const unsigned l16 = lane * 16;
    const bool gemm_blk = idx < 28;
    const int mt2 = 4 * g8 + (gemm_blk ? idx / 7 : 0), ng = gemm_blk ? idx % 7 : 0;
    const int pn1 = 2 * g8 + (gemm_blk ? idx / 14 : 0), cg = gemm_blk ? idx % 14 : 0;
    const int pnl = 2 * g8 + (idx >> 4), head = idx & 15;
    const int mt = pnl * 4 + wm;
    char* sW = smem;
    char* sKV = smem + R1;
    const int i16 = lane & 15;

    if (b < 42) {
        float* sRed = (float*)(smem + 88 * 1024);
        u32x4 mw[7];
#pragma unroll
        for (int i = 0; i < 7; i++) mw[i] = *(const u32x4*)(p.wmod + (size_t)(56 * w + 8 * i + (lane >> 3)) * 2688 + b * 64 + (lane & 7) * 8);
        const float c0 = bf2f(p.c[56 * w + (lane < 56 ? lane : 0)]);
        SB();
        const float s0 = rbf(c0 / (1.f + __expf(-c0)));
        f32x2 acc[4] = {{0.f, 0.f}, {0.f, 0.f}, {0.f, 0.f}, {0.f, 0.f}};
#pragma unroll
        for (int i = 0; i < 7; i++) {
            const float sv = __shfl(s0, 8 * i + (lane >> 3));
#pragma unroll
            for (int j = 0; j < 4; j++) acc[j] = fma2(up2(mw[i][j]), f32x2{sv, sv}, acc[j]);
        }
        *(f32x4*)(sRed + (w * 8 + (lane >> 3)) * 64 + (lane & 7) * 8) = f32x4{acc[0][0], acc[0][1], acc[1][0], acc[1][1]};
        *(f32x4*)(sRed + (w * 8 + (lane >> 3)) * 64 + (lane & 7) * 8 + 4) = f32x4{acc[2][0], acc[2][1], acc[3][0], acc[3][1]};
        float s = 0.f;
#pragma unroll
        for (int i = 0; i < 8; i++) s += sRed[(w * 8 + i) * 64 + lane];
        sRed[4096 + w * 64 + lane] = s;
        __syncthreads();
        if (w == 7) {
            s = 0.f;
#pragma unroll
            for (int i = 0; i < 8; i++) s += sRed[4096 + i * 64 + lane];
            s = rbf(s);
            if ((b >= 7 && b < 14) || (b >= 28 && b < 35)) s = rbf(1.f + s);
            if (b < 14) {
                const u64 gr = ((u64)(__float_as_uint(s) >> 16) << 48) | (tag & 0xffffffffffffull);
                __hip_atomic_store((u64*)(p.ws + OFF_MODG) + b * 64 + lane, gr, RLX_AGENT);
            } else {
                stf_sc1(s, rs, lane * 4, OFF_MODP + b * 256);
                drain();
                set_flag(fl, FL_MOD + b, lane, tag);
            }
        }
        if (b < 14) __syncthreads();
    }
    float touch0 = 0.f;
    if (w == 0) {
        char* scr = smem + R1;
        const int r = lane & 7, q = lane >> 3;
        const char* xs = (const char*)(p.x + (size_t)(b * 8 + r) * D_) + q * 16;
        u32x4 v[7];
#pragma unroll
        for (int i = 0; i < 7; i++) v[i] = *(const u32x4*)(xs + i * 128);
        SB();
        f32x2 xf[28];
        f32x2 s2 = {0.f, 0.f};
#pragma unroll
        for (int i = 0; i < 7; i++)
#pragma unroll
            for (int j = 0; j < 4; j++) { xf[i * 4 + j] = up2(v[i][j]); s2 += xf[i * 4 + j]; }
        float sum = s2[0] + s2[1];
        sum += __shfl_xor(sum, 8); sum += __shfl_xor(sum, 16); sum += __shfl_xor(sum, 32);
        const float mean = sum * (1.f / D_);
        f32x2 q2 = {0.f, 0.f};
#pragma unroll
        for (int i = 0; i < 28; i++) { xf[i] -= f32x2{mean, mean}; q2 = fma2(xf[i], xf[i], q2); }
        float m2 = q2[0] + q2[1];
        m2 += __shfl_xor(m2, 8); m2 += __shfl_xor(m2, 16); m2 += __shfl_xor(m2, 32);
        const float rstd = rsqrtf(m2 * (1.f / D_) + 1e-5f);
#pragma unroll
        for (int i = 0; i < 28; i++) xf[i] *= f32x2{rstd, rstd};
        {
            const u64* gp = (const u64*)(p.ws + OFF_MODG) + lane;
            float* sg = (float*)scr;
#pragma nounroll
            for (int spins = 0; spins < (1 << 20); spins++) {
                u64 gv[14];
#pragma unroll
                for (int i = 0; i < 14; i++) gv[i] = __hip_atomic_load(gp + i * 64, RLX_AGENT);
                bool ok = true;
#pragma unroll
                for (int i = 0; i < 14; i++) { ok = ok && ((gv[i] ^ tag) << 16) == 0; sg[i * 64 + lane] = __uint_as_float((unsigned)(gv[i] >> 32) & 0xffff0000u); }
                if (__all(ok)) break;
                __builtin_amdgcn_s_sleep(1);
            }
        }
        const unsigned dst = OFF_XF + (b >> 2) * KS_D * 1024 + (q >> 1) * 1024 + ((q & 1) * 32 + (b & 3) * 8 + r) * 16;
#pragma unroll
        for (int i = 0; i < 7; i++) {
            const f32x4 g0 = *(const f32x4*)(scr + 1792 + q * 32 + i * 256), g1 = *(const f32x4*)(scr + 1792 + q * 32 + 16 + i * 256);
            const f32x4 s0 = *(const f32x4*)(scr + q * 32 + i * 256), s1 = *(const f32x4*)(scr + q * 32 + 16 + i * 256);
            u32x4 o;
            o[0] = pk2v(fma2(xf[4 * i + 0], f32x2{g0[0], g0[1]}, f32x2{s0[0], s0[1]}));
            o[1] = pk2v(fma2(xf[4 * i + 1], f32x2{g0[2], g0[3]}, f32x2{s0[2], s0[3]}));
            o[2] = pk2v(fma2(xf[4 * i + 2], f32x2{g1[0], g1[1]}, f32x2{s1[0], s1[1]}));
            o[3] = pk2v(fma2(xf[4 * i + 3], f32x2{g1[2], g1[3]}, f32x2{s1[2], s1[3]}));
            stb_sc1(o, rs, dst, i * 4096);
        }
        drain();
        set_flag(fl, FL_XF + b, lane, tag);
    } else {
        const int rl = lane / 15, pc = lane % 15, part = pc / 5, pp = pc % 5;
        const int odd = head & 1;
        const unsigned col0 = part * 896 + head * 56 - odd * 8 + pp * 16;
        const char* wbase = (const char*)p.wqkv;
        char* dst = sW + (64 * (w - 1) + rl) * 192 + part * 64 + pp * 16 - odd * 8;
        {
            u32x4 v[16];
#pragma unroll
            for (int i = 0; i < 16; i++) {
                unsigned off = (unsigned)(64 * (w - 1) + i * 4 + (rl < 4 ? rl : 0)) * 2688 + col0;
                off = off < 448u * 2688u - 16u ? off : 448u * 2688u - 16u;
                v[i] = *(const u32x4*)(wbase + off);
            }
            if (w >= 6) {
                const unsigned ln = (b * 2 + (w - 6)) * 64 + lane;
                const char* tp = ln < 3136 ? (const char*)p.wo + ln * 128 : (ln < 15680 ? (const char*)p.w1 + (ln - 3136) * 128 : (const char*)p.w2 + (ln < 28224 ? ln - 15680 : 0) * 128);
                touch0 = *(const float*)tp;
            }
            SB();
            if (rl < 4) {
#pragma unroll
                for (int i = 0; i < 16; i++) {
                    char* d = dst + i * 4 * 192;
                    if (odd) {
                        if (pp > 0) *(u32x2*)(d) = u32x2{v[i][0], v[i][1]};
                        if (pp < 4) *(u32x2*)(d + 8) = u32x2{v[i][2], v[i][3]};
                    } else if (pp < 4) *(u32x4*)(d) = v[i];
                }
            }
            SB();
        }
    }
    f32x16 ak = {0}, av = {0};
    u32x4 wo0[8], wo1[8];
    u32x4 a[14];
    const char* bw = sW + (wh * 14 * 16 + 8 * h + (i16 >> 2)) * 192 + ((lane >> 4) & 1) * 32 + (i16 & 3) * 8;
    {
        const unsigned srcA = OFF_XF + (mt * KS_D + wh * 14) * 1024;
        wait_flags(fl, lane < 4 ? FL_XF + mt * 4 + lane : -1, tag);
#pragma unroll
        for (int i = 0; i < 8; i++) a[i] = LDF1(srcA, i);
        SB();
        asm volatile("" :: "v"(touch0));
        __syncthreads();
        u32x2 f[3][4];
#define LDW(i) do { _Pragma("unroll") for (int q = 0; q < 2; q++) { f[(i) % 3][2 * q] = trd(bw + (i) * 3072 + 64 + q * 64); f[(i) % 3][2 * q + 1] = trd(bw + (i) * 3072 + 768 + 64 + q * 64); } } while (0)
        LDW(0); LDW(1);
        SB();
#pragma unroll
        for (int i = 0; i < 14; i++) {
            if (i + 2 < 14) LDW(i + 2);
            if (i + 8 < 14) a[i + 8] = LDF1(srcA, i + 8);
            SB();
            const u32x4 fk = {f[i % 3][0][0], f[i % 3][0][1], f[i % 3][1][0], f[i % 3][1][1]};
            const u32x4 fv = {f[i % 3][2][0], f[i % 3][2][1], f[i % 3][3][0], f[i % 3][3][1]};
            ak = MFMA(fk, a[i], ak);
            av = MFMA(a[i], fv, av);
            SB();
        }
    }
    if (wh) st_tile(sKV + w * 4096 + l16, ak); else st_tile(sKV + w * 4096 + l16, av);
    __syncthreads();
    {
        u32x4 o0, o1;
        if (wh == 0) {
            add_tile(sKV + (w ^ 4) * 4096 + l16, ak);
#pragma unroll
            for (int j = 0; j < 4; j++) { o0[j] = pk2(ak[2 * j], ak[2 * j + 1]); o1[j] = pk2(ak[8 + 2 * j], ak[8 + 2 * j + 1]); }
            if (h) { o1[2] = 0; o1[3] = 0; }
        } else {
            add_tile(sKV + (w ^ 4) * 4096 + l16, av);
            o0[0] = pk2(av[0], av[1]); o0[1] = pk2(av[2], av[3]); o0[2] = pk2(av[8], av[9]); o0[3] = pk2(av[10], av[11]);
            o1[0] = pk2(av[4], av[5]); o1[1] = pk2(av[6], av[7]); o1[2] = pk2(av[12], av[13]); o1[3] = pk2(av[14], av[15]);
            if (c >= DH) { const unsigned z = c == DH ? 0x3f803f80u : 0u; o0 = u32x4{z, z, z, z}; o1 = o0; }
        }
        const unsigned lo = wh * 2048 + (c >> 4) * 1024 + h * 256 + (c & 15) * 16;
        stb_sc1(o0, rs, lo, OFF_KV + (head * 64 + mt) * 4096);
        stb_sc1(o1, rs, lo + 512, OFF_KV + (head * 64 + mt) * 4096);
        *(u32x4*)(sKV + (8 + wm) * 4096 + lo) = o0;
        *(u32x4*)(sKV + (8 + wm) * 4096 + lo + 512) = o1;
    }
    __syncthreads();
    u32x4 qf;
    {
        f32x16 aq = {0};
        u32x2 f[3][2];
#define LDQ(i) do { f[(i) % 3][0] = trd(bw + (i) * 3072); f[(i) % 3][1] = trd(bw + (i) * 3072 + 768); } while (0)
        LDQ(0); LDQ(1);
        SB();
#pragma unroll
        for (int i = 0; i < 14; i++) {
            if (i + 2 < 14) LDQ(i + 2);
            SB();
            aq = MFMA((u32x4{f[i % 3][0][0], f[i % 3][0][1], f[i % 3][1][0], f[i % 3][1][1]}), a[i], aq);
            SB();
            if (i == 5) { drain(); set_flag(fl, FL_KV + (head * 64 + mt) * 2 + wh, lane, tag); SB(); }
        }
        char* qx = smem + 88 * 1024;
        if ((c >> 4) != wh) {
#pragma unroll
            for (int q = 0; q < 4; q++) *(f32x4*)(qx + w * 2048 + q * 512 + (h * 16 + (c & 15)) * 16) = f32x4{aq[4 * q], aq[4 * q + 1], aq[4 * q + 2], aq[4 * q + 3]};
        } else {
#pragma unroll
            for (int q = 0; q < 4; q++) *(f32x4*)(sKV + w * 4096 + q * 512 + (h * 16 + (c & 15)) * 16 + 2048) = f32x4{aq[4 * q], aq[4 * q + 1], aq[4 * q + 2], aq[4 * q + 3]};
        }
        __syncthreads();
        const int g4 = lane >> 4, s = g4 >> 1, hq = g4 & 1;
        const char* p0 = qx + (w ^ 4) * 2048 + (2 * s) * 512 + (hq * 16 + i16) * 16;
        const char* p1 = sKV + w * 4096 + 2048 + (2 * s) * 512 + (hq * 16 + i16) * 16;
        const f32x4 x0 = *(const f32x4*)(p0), x1 = *(const f32x4*)(p0 + 512);
        const f32x4 y0 = *(const f32x4*)(p1), y1 = *(const f32x4*)(p1 + 512);
        const float qs = 0.18898223650461362f * 1.4426950408889634f;
        const f32x4 z0 = (x0 + y0) * qs, z1 = (x1 + y1) * qs;
        qf[0] = pk2(z0[0], z0[1]); qf[1] = pk2(z0[2], z0[3]); qf[2] = pk2(z1[0], z1[1]); qf[3] = pk2(z1[2], z1[3]);
        if (g4 == 3) { qf[2] = 0; qf[3] = 0; }
    }
    u32x4 w1r[14];
    const u16* w1p = p.w1 + (size_t)(56 * w + (lane >> 4)) * DFF + cg * 128 + i16 * 8;
    const int rr = lane >> 3;
    const u16* wop = p.wo + (size_t)(wm * 4 * DH + rr) * D_ + ng * 64 + (lane & 7) * 8;
    {
        const int kt = pnl * 4 - 8 + w;
        wait_flags(fl, lane < 2 ? (kt >= 0 ? FL_KV + (head * 64 + kt) * 2 + lane : -1) : (lane < 5 ? FL_MOD + (lane == 2 ? 14 : lane == 3 ? 28 : 35) + ng : -1), tag);
        if (kt >= 0) {
            u32x4 kv[4];
#pragma unroll
            for (int i = 0; i < 4; i++) kv[i] = ldb_sc1(rs, l16 + i * 1024, OFF_KV + (head * 64 + kt) * 4096);
#pragma unroll
            for (int i = 0; i < 4; i++) *(u32x4*)(sKV + w * 4096 + i * 1024 + l16) = kv[i];
        } else {
#pragma unroll
            for (int i = 0; i < 4; i++) *(u32x4*)(sKV + w * 4096 + i * 1024 + l16) = u32x4{0, 0, 0, 0};
        }
        __syncthreads();
        const int T = mt;
        const char* kvb = sKV + wm * 4096 + l16;
        const int g4 = lane >> 4, ql = wh * 16 + i16;
        f32x4 s[18];
        const float NEG = -1e30f;
        u32x4 kf[3][2];
        kf[0][0] = *(const u32x4*)(kvb); kf[0][1] = *(const u32x4*)(kvb + 1024);
        kf[1][0] = *(const u32x4*)(kvb + 4096); kf[1][1] = *(const u32x4*)(kvb + 4096 + 1024);
        f32x4 mlo[2], mhi[2];
#pragma unroll
        for (int u = 0; u < 2; u++)
#pragma unroll
            for (int r = 0; r < 4; r++) {
                const bool gt = (16 * u + 4 * g4 + r) > ql;
                mlo[u][r] = gt ? 0.f : NEG; mhi[u][r] = gt ? NEG : 0.f;
            }
        SB();
#pragma unroll
        for (int i = 0; i < 9; i++) {
            if (i + 2 < 9) { kf[(i + 2) % 3][0] = *(const u32x4*)(kvb + (i + 2) * 4096); kf[(i + 2) % 3][1] = *(const u32x4*)(kvb + (i + 2) * 4096 + 1024); }
            if (i < 8) {
                const int hd = i >> 1, sx = i & 1;
                wo0[i] = *(const u32x4*)(wop + (size_t)(hd * DH + 8 * sx) * D_);
                wo1[i] = *(const u32x4*)(wop + (size_t)(hd * DH + 8 * sx + ((sx == 1 && rr >= 4) ? 0 : 16)) * D_);
            }
            SB();
            const f32x4 z = {0, 0, 0, 0};
            s[2 * i] = MFMA16(kf[i % 3][0], qf, i == 0 ? mlo[0] : (i == 8 ? mhi[0] : z));
            s[2 * i + 1] = MFMA16(kf[i % 3][1], qf, i == 0 ? mlo[1] : (i == 8 ? mhi[1] : z));
            SB();
        }
        u32x4 vf[2][2];
        vf[0][0] = *(const u32x4*)(kvb + 2048); vf[0][1] = *(const u32x4*)(kvb + 3072);
        SB();
        if (__builtin_expect(T < 8, 0)) {
#pragma unroll
            for (int i = 0; i < 8; i++)
                if (T - 8 + i < 0) { s[2 * i] = f32x4{NEG, NEG, NEG, NEG}; s[2 * i + 1] = f32x4{NEG, NEG, NEG, NEG}; asm volatile("" : "+v"(s[2 * i]), "+v"(s[2 * i + 1])); }
        }
        float m0 = fmaxf(s[0][0], s[0][1]), m1 = fmaxf(s[0][2], s[0][3]);
#pragma unroll
        for (int i = 1; i < 18; i++) { m0 = fmaxf(fmaxf(m0, s[i][0]), s[i][1]); m1 = fmaxf(fmaxf(m1, s[i][2]), s[i][3]); }
        float m = fmaxf(m0, m1);
        m = fmaxf(m, __shfl_xor(m, 16));
        m = fmaxf(m, __shfl_xor(m, 32));
        f32x4 o0 = {0, 0, 0, 0}, o1 = {0, 0, 0, 0};
        SB();
#pragma unroll
        for (int i = 0; i < 9; i++) {
            if (i + 1 < 9) { vf[(i + 1) & 1][0] = *(const u32x4*)(kvb + (i + 1) * 4096 + 2048); vf[(i + 1) & 1][1] = *(const u32x4*)(kvb + (i + 1) * 4096 + 3072); }
            if (i < 7) { w1r[2 * i] = *(const u32x4*)(w1p + (size_t)(8 * i) * DFF); w1r[2 * i + 1] = *(const u32x4*)(w1p + (size_t)(8 * i + 4) * DFF); }
            u32x4 pf;
            pf[0] = pk2(__builtin_amdgcn_exp2f(s[2 * i][0] - m), __builtin_amdgcn_exp2f(s[2 * i][1] - m));
            pf[1] = pk2(__builtin_amdgcn_exp2f(s[2 * i][2] - m), __builtin_amdgcn_exp2f(s[2 * i][3] - m));
            pf[2] = pk2(__builtin_amdgcn_exp2f(s[2 * i + 1][0] - m), __builtin_amdgcn_exp2f(s[2 * i + 1][1] - m));
            pf[3] = pk2(__builtin_amdgcn_exp2f(s[2 * i + 1][2] - m), __builtin_amdgcn_exp2f(s[2 * i + 1][3] - m));
            SB();
            o0 = MFMA16(vf[i & 1][0], pf, o0);
            o1 = MFMA16(vf[i & 1][1], pf, o1);
            SB();
        }
        const float l = __shfl(o1[0], 48 + i16);
        const float inv = __builtin_amdgcn_rcpf(l);
        o0 *= inv; o1 *= inv;
        u32x4 ov;
        ov[0] = pk2(o0[0], o0[1]); ov[1] = pk2(o0[2], o0[3]); ov[2] = pk2(o1[0], o1[1]); ov[3] = pk2(o1[2], o1[3]);
        stb_sc1(ov, rs, (g4 >> 1) * 1024 + ((g4 & 1) * 32 + ql) * 16, OFF_O + (T * KS_O + head * 2) * 1024);
        drain();
        set_flag(fl, FL_O + T * 32 + head * 2 + wh, lane, tag);
    }
    if (!gemm_blk) {
        const int gb = (idx - 28) * 8 + g8;
        if (gb < 28) {
            float* sRed = (float*)smem;
            u32x4 mw[7];
#pragma unroll
            for (int i = 0; i < 7; i++) mw[i] = *(const u32x4*)(p.w1 + (size_t)(56 * w + 8 * i + (lane >> 3)) * DFF + gb * 64 + (lane & 7) * 8);
            wait_flags(fl, lane < 14 ? FL_MOD + 21 + lane : -1, tag);
            const unsigned kk = (56 * w + (lane < 56 ? lane : 0)) * 4;
            const float gk = ldf_sc1(rs, kk, OFF_MODP + 1792 * 4), sk = ldf_sc1(rs, kk, OFF_MODP + 1344 * 4);
            f32x2 ag[4] = {{0.f, 0.f}, {0.f, 0.f}, {0.f, 0.f}, {0.f, 0.f}}, ab[4] = {{0.f, 0.f}, {0.f, 0.f}, {0.f, 0.f}, {0.f, 0.f}};
#pragma unroll
            for (int i = 0; i < 7; i++) {
                const float gv = __shfl(gk, 8 * i + (lane >> 3)), sv = __shfl(sk, 8 * i + (lane >> 3));
#pragma unroll
                for (int j = 0; j < 4; j++) { const f32x2 e = up2(mw[i][j]); ag[j] = fma2(e, f32x2{gv, gv}, ag[j]); ab[j] = fma2(e, f32x2{sv, sv}, ab[j]); }
            }
            __syncthreads();
            float* pr = sRed + (w * 8 + (lane >> 3)) * 128 + (lane & 7) * 8;
            *(f32x4*)(pr) = f32x4{ag[0][0], ag[0][1], ag[1][0], ag[1][1]}; *(f32x4*)(pr + 4) = f32x4{ag[2][0], ag[2][1], ag[3][0], ag[3][1]};
            *(f32x4*)(pr + 64) = f32x4{ab[0][0], ab[0][1], ab[1][0], ab[1][1]}; *(f32x4*)(pr + 68) = f32x4{ab[2][0], ab[2][1], ab[3][0], ab[3][1]};
            float s0 = 0.f, s1 = 0.f;
#pragma unroll
            for (int i = 0; i < 8; i++) { s0 += sRed[(w * 8 + i) * 128 + lane]; s1 += sRed[(w * 8 + i) * 128 + 64 + lane]; }
            sRed[8192 + w * 128 + lane] = s0; sRed[8192 + w * 128 + 64 + lane] = s1;
            __syncthreads();
            if (w == 0) {
                s0 = 0.f; s1 = 0.f;
#pragma unroll
                for (int i = 0; i < 8; i++) { s0 += sRed[8192 + i * 128 + lane]; s1 += sRed[8192 + i * 128 + 64 + lane]; }
                stf_sc1(s0, rs, lane * 4, OFF_GB2 + gb * 512);
                stf_sc1(s1, rs, lane * 4 + 256, OFF_GB2 + gb * 512);
                drain();
                set_flag(fl, FL_GB2 + gb, lane, tag);
            }
        }
        return;
    }

    const int umt = 2 * mt2 + wh, unt = 2 * ng + (wm >> 1), us = wm & 1;
    u32x2 xr[2];
    f32x4 gav[2], gbv[2], g2v[2];
    u32x4 w2a[14], w2b[14];
    const u16* w2p = p.w2 + (size_t)(224 * w + (lane >> 3)) * D_ + ng * 64 + (lane & 7) * 8;
#define LDW2(i) do { w2a[i] = *(const u32x4*)(w2p + (size_t)(16 * (i)) * D_); w2b[i] = *(const u32x4*)(w2p + (size_t)(16 * (i) + 8) * D_); } while (0)
    {
        const u16* xrow = p.x + (size_t)(umt * 32 + c) * D_ + unt * 32 + 16 * us + 4 * h;
        xr[0] = *(const u32x2*)(xrow); xr[1] = *(const u32x2*)(xrow + 8);
        const unsigned no = (unt * 32 + 16 * us) * 4;
        gav[0] = ldf4_sc1(rs, h * 16, OFF_MODP + 896 * 4 + no); gav[1] = ldf4_sc1(rs, h * 16 + 32, OFF_MODP + 896 * 4 + no);
        gbv[0] = ldf4_sc1(rs, h * 16, OFF_MODP + 2240 * 4 + no); gbv[1] = ldf4_sc1(rs, h * 16 + 32, OFF_MODP + 2240 * 4 + no);
        g2v[0] = ldf4_sc1(rs, h * 16, OFF_MODP + 1792 * 4 + no); g2v[1] = ldf4_sc1(rs, h * 16 + 32, OFF_MODP + 1792 * 4 + no);
        LDW2(0);
        SB();
        char* scr = smem + 40960 + w * 4096;
#pragma unroll
        for (int k = 0; k < 8; k++) {
            xpose2(scr, lane, wo0[k], wo1[k]);
            if ((k & 1) && h) { wo0[k][2] = 0; wo0[k][3] = 0; wo1[k][2] = 0; wo1[k][3] = 0; }
        }
    }
    wait_flags(fl, lane < 8 ? FL_O + umt * 32 + 8 * wm + lane : -1, tag);
    u32x4 x1p;
    {
        u32x4 a0[8];
#pragma unroll
        for (int i = 0; i < 8; i++) a0[i] = LDF1(OFF_O + (umt * KS_O + wm * 8) * 1024, i);
        SB();
        f32x16 c0 = {0}, c1 = {0};
#pragma unroll
        for (int i = 0; i < 8; i++) {
            c0 = MFMA(wo0[i], a0[i], c0);
            c1 = MFMA(wo1[i], a0[i], c1);
            SB();
            if (i == 0) LDW2(1);
            if (i == 2) LDW2(2);
            if (i == 4) LDW2(3);
            if (i == 6) LDW2(4);
            SB();
        }
        __syncthreads();
        char* red = smem + R1 + wh * 24576;
        f32x4 r0, r1;
#define WR_HU(hu, cc, o) do { const int slot = (hu) * 3 + (wm > (hu) ? wm - 1 : wm); \
            *(f32x4*)(red + slot * 2048 + l16) = f32x4{cc[o], cc[o + 1], cc[o + 2], cc[o + 3]}; \
            *(f32x4*)(red + slot * 2048 + 1024 + l16) = f32x4{cc[o + 4], cc[o + 5], cc[o + 6], cc[o + 7]}; } while (0)
#define KP_HU(cc, o) do { r0 = f32x4{cc[o], cc[o + 1], cc[o + 2], cc[o + 3]}; r1 = f32x4{cc[o + 4], cc[o + 5], cc[o + 6], cc[o + 7]}; } while (0)
        if (wm == 0) { WR_HU(1, c0, 8); WR_HU(2, c1, 0); WR_HU(3, c1, 8); KP_HU(c0, 0); }
        else if (wm == 1) { WR_HU(0, c0, 0); WR_HU(2, c1, 0); WR_HU(3, c1, 8); KP_HU(c0, 8); }
        else if (wm == 2) { WR_HU(0, c0, 0); WR_HU(1, c0, 8); WR_HU(3, c1, 8); KP_HU(c1, 0); }
        else { WR_HU(0, c0, 0); WR_HU(1, c0, 8); WR_HU(2, c1, 0); KP_HU(c1, 8); }
        __syncthreads();
#pragma unroll
        for (int s3 = 0; s3 < 3; s3++) {
            r0 += *(const f32x4*)(red + (wm * 3 + s3) * 2048 + l16);
            r1 += *(const f32x4*)(red + (wm * 3 + s3) * 2048 + 1024 + l16);
        }
        x1p[0] = pk2v(fma2(f32x2{gav[0][0], gav[0][1]}, f32x2{r0[0], r0[1]}, up2(xr[0][0])));
        x1p[1] = pk2v(fma2(f32x2{gav[0][2], gav[0][3]}, f32x2{r0[2], r0[3]}, up2(xr[0][1])));
        x1p[2] = pk2v(fma2(f32x2{gav[1][0], gav[1][1]}, f32x2{r1[0], r1[1]}, up2(xr[1][0])));
        x1p[3] = pk2v(fma2(f32x2{gav[1][2], gav[1][3]}, f32x2{r1[2], r1[3]}, up2(xr[1][1])));
        f32x2 xv[4];
#pragma unroll
        for (int j = 0; j < 4; j++) xv[j] = up2(x1p[j]);
        u32x4 xg;
        xg[0] = pk2v(xv[0] * f32x2{g2v[0][0], g2v[0][1]}); xg[1] = pk2v(xv[1] * f32x2{g2v[0][2], g2v[0][3]});
        xg[2] = pk2v(xv[2] * f32x2{g2v[1][0], g2v[1][1]}); xg[3] = pk2v(xv[3] * f32x2{g2v[1][2], g2v[1][3]});
        stb_sc1(xg, rs, l16, OFF_X1 + (umt * KS_D + 2 * unt + us) * 1024);
        const f32x2 s2 = (xv[0] + xv[1]) + (xv[2] + xv[3]);
        float sum = s2[0] + s2[1];
        sum += __shfl_xor(sum, 32);
        const float mean = sum * (1.f / 16.f);
        f32x2 q2 = {0.f, 0.f};
#pragma unroll
        for (int j = 0; j < 4; j++) { const f32x2 d = xv[j] - f32x2{mean, mean}; q2 = fma2(d, d, q2); }
        float m2 = q2[0] + q2[1];
        m2 += __shfl_xor(m2, 32);
        if (h == 0) *(f32x2*)(red + wm * 6144 + c * 8) = f32x2{mean, m2};
        drain();
        set_flag(fl, FL_X1 + umt * 32 + 2 * unt + us, lane, tag);
        SB();
    }
    char* scr2 = smem + R1 + w * 6144 + 1024;
    LDW2(5); LDW2(6); LDW2(7); LDW2(8);
    SB();
    {
        char* d = smem + (56 * w + (lane >> 4)) * 256 + ((i16 ^ ((lane >> 4) << 2)) * 16);
#pragma unroll
        for (int i = 0; i < 14; i++) *(u32x4*)(d + i * 1024) = w1r[i];
    }
    __syncthreads();
    float* sGB = (float*)(smem + R1 + wh * 24576 + 6144 + 4096);
    if (wm == 0) {
        const f32x2* sp = (const f32x2*)(smem + R1 + wh * 24576) + c;
        const f32x2 a0 = sp[0], a1 = sp[768], a2 = sp[1536], a3 = sp[2304];
        const float mu = 0.25f * ((a0[0] + a1[0]) + (a2[0] + a3[0]));
        const float d0 = a0[0] - mu, d1 = a1[0] - mu, d2 = a2[0] - mu, d3 = a3[0] - mu;
        const float m2 = (a0[1] + a1[1]) + (a2[1] + a3[1]) + 16.f * ((d0 * d0 + d1 * d1) + (d2 * d2 + d3 * d3));
        if (h == 0) stf2_sc1(f32x2{mu, m2}, rs, c * 8, OFF_ST2 + (umt * 8 + ng) * 256);
        drain();
        set_flag(fl, FL_ST + umt * 8 + ng, lane, tag);
    } else if (wm == 1) {
        wait_flags(fl, lane == 0 ? FL_GB2 + cg * 2 + wh : -1, tag);
        const float g = ldf_sc1(rs, lane * 4, OFF_GB2 + (cg * 2 + wh) * 512), bb = ldf_sc1(rs, lane * 4 + 256, OFF_GB2 + (cg * 2 + wh) * 512);
        sGB[lane] = g; sGB[64 + lane] = bb;
    }
    #pragma unroll
    for (int i = 0; i < 5; i++) xpose2(scr2, lane, w2a[i], w2b[i]);
    const int m1 = pn1 * 4 + wm;
    wait_flags(fl, lane < 28 ? FL_X1 + m1 * 32 + lane : -1, tag);
    {
        constexpr int PX = 6;
        u32x4 xa[KS_D];
        const unsigned xs = OFF_X1 + m1 * KS_D * 1024;
#pragma unroll
        for (int i = 0; i < PX; i++) xa[i] = LDF1(xs, i);
        SB();
        f32x16 c0 = {0}, c1 = {0};
        u32x2 fb[3][4];
        const int q4 = i16 >> 2;
        const char* bl0 = smem + (4 * h + q4) * 256 + (((8 * wh + 2 * ((lane >> 4) & 1) + ((i16 & 3) >> 1)) ^ (q4 << 2)) * 16) + (i16 & 1) * 8;
        const char* bl1 = smem + (4 * h + q4) * 256 + (((8 * wh + 4 + 2 * ((lane >> 4) & 1) + ((i16 & 3) >> 1)) ^ (q4 << 2)) * 16) + (i16 & 1) * 8;
        const char* bh0 = bl0 + 57344; const char* bh1 = bl1 + 57344;
#define LDFB(S) do { if ((S) < KS_D) { const char* p0_ = (S) < 14 ? bl0 + (S) * 4096 : bh0 + ((S) - 14) * 4096; const char* p1_ = (S) < 14 ? bl1 + (S) * 4096 : bh1 + ((S) - 14) * 4096; \
        fb[(S) % 3][0] = trd(p0_); fb[(S) % 3][1] = trd(p0_ + 2048); fb[(S) % 3][2] = trd(p1_); fb[(S) % 3][3] = trd(p1_ + 2048); } } while (0)
        LDFB(0); LDFB(1);
        SB();
        f32x2 st2[4];
        u64 stf = 0;
        __syncthreads();
#pragma unroll
        for (int S = 0; S < KS_D; S++) {
            LDFB(S + 2);
            SB();
            c0 = MFMA((u32x4{fb[S % 3][0][0], fb[S % 3][0][1], fb[S % 3][1][0], fb[S % 3][1][1]}), xa[S], c0);
            SB();
            if (S + PX < KS_D) xa[S + PX] = LDF1(xs, S + PX);
            if (S == 14) stf = __hip_atomic_load(fl + (FL_ST + m1 * 8 + (lane < 7 ? lane : 0)) * 16, RLX_AGENT);
            if (S == 22) {
                if (!__all(stf == tag)) wait_flags(fl, lane < 7 ? FL_ST + m1 * 8 + lane : -1, tag);
#pragma unroll
                for (int i = 0; i < 4; i++) st2[i] = ldf2_sc1(rs, (h * 4 + i < 7 ? h * 4 + i : 6) * 256 + c * 8, OFF_ST2 + m1 * 8 * 256);
            }
            if (S >= 23) { LDW2(S - 14); }
            SB();
            c1 = MFMA((u32x4{fb[S % 3][2][0], fb[S % 3][2][1], fb[S % 3][3][0], fb[S % 3][3][1]}), xa[S], c1);
            SB();
        }
        float rsd, nmr;
        {
            const float wl = h ? 0.f : 1.f;
            float s = (st2[0][0] + st2[1][0]) + (st2[2][0] + wl * st2[3][0]);
            s += __shfl_xor(s, 32);
            const float mu = s * (1.f / 7.f);
            const float d0 = st2[0][0] - mu, d1 = st2[1][0] - mu, d2 = st2[2][0] - mu, d3 = st2[3][0] - mu;
            float m2 = (st2[0][1] + st2[1][1]) + (st2[2][1] + wl * st2[3][1]) + 64.f * ((d0 * d0 + d1 * d1) + (d2 * d2 + wl * d3 * d3));
            m2 += __shfl_xor(m2, 32);
            rsd = rsqrtf(m2 * (1.f / D_) + 1e-5f);
            nmr = -mu * rsd;
        }
        const f32x2 rs2 = {rsd, rsd}, nm2 = {nmr, nmr};
#define EPI_Q(e, n, a4, dst) do { \
        const f32x4 G = *(const f32x4*)(sGB + (n) * 32 + 8 * (a4) + 4 * h), B = *(const f32x4*)(sGB + 64 + (n) * 32 + 8 * (a4) + 4 * h); \
        dst[((a4) & 1) * 2] = relu_pk(pk2v(fma2(rs2, f32x2{e[4 * (a4)], e[4 * (a4) + 1]}, fma2(nm2, f32x2{G[0], G[1]}, f32x2{B[0], B[1]})))); \
        dst[((a4) & 1) * 2 + 1] = relu_pk(pk2v(fma2(rs2, f32x2{e[4 * (a4) + 2], e[4 * (a4) + 3]}, fma2(nm2, f32x2{G[2], G[3]}, f32x2{B[2], B[3]})))); } while (0)
        u32x4 eo[4];
#pragma unroll
        for (int a4 = 0; a4 < 4; a4++) { EPI_Q(c0, 0, a4, eo[a4 >> 1]); }
#pragma unroll
        for (int a4 = 0; a4 < 4; a4++) { EPI_Q(c1, 1, a4, eo[2 + (a4 >> 1)]); }
#pragma unroll
        for (int i = 0; i < 4; i++) stb_sc1(eo[i], rs, l16, OFF_H1 + (m1 * KS_F + (cg * 4 + 2 * wh) * 2 + i) * 1024);
        drain();
        set_flag(fl, FL_H1 + m1 * 32 + cg * 2 + wh, lane, tag);
        SB();
#pragma unroll
        for (int i = 5; i < 14; i++) xpose2(scr2, lane, w2a[i], w2b[i]);
        __syncthreads();
    }
    {
        const int f0 = (7 * w) >> 1, f1 = (7 * w + 6) >> 1;
        const int fi = f0 + (lane & 3);
        wait_flags(fl, (lane < 8 && fi <= f1) ? FL_H1 + (2 * mt2 + (lane >> 2)) * 32 + fi : -1, tag);
    }
    f32x16 acc[4];
    {
        constexpr int PD = 5;
        u32x4 a0[14], a1[14];
        const unsigned s0 = OFF_H1 + ((2 * mt2) * KS_F + w * 14) * 1024, s1 = s0 + KS_F * 1024;
#define LDH(i) do { a0[i] = ldb_sc1(rs, l16 + ((i) & 1) * 1024, s0 + ((i) >> 1) * 2048); a1[i] = ldb_sc1(rs, l16 + ((i) & 1) * 1024, s1 + ((i) >> 1) * 2048); } while (0)
#pragma unroll
        for (int i = 0; i < PD; i++) LDH(i);
        SB();
#pragma unroll
        for (int u = 0; u < 4; u++) acc[u] = f32x16{0};
#pragma unroll
        for (int i = 0; i < 14; i++) {
            acc[0] = MFMA(w2a[i], a0[i], acc[0]);
            acc[1] = MFMA(w2b[i], a0[i], acc[1]);
            SB();
            if (i + PD < 14) LDH(i + PD);
            SB();
            acc[2] = MFMA(w2a[i], a1[i], acc[2]);
            acc[3] = MFMA(w2b[i], a1[i], acc[3]);
            SB();
        }
    }
    {
        char* red = smem + w * 2048 + l16;
#pragma unroll
        for (int hu = 0; hu < 8; hu++) {
            const f32x16& cc = acc[hu >> 1];
            const int r0 = (hu & 1) * 8;
            *(f32x4*)(red + hu * 16384) = f32x4{cc[r0], cc[r0 + 1], cc[r0 + 2], cc[r0 + 3]};
            *(f32x4*)(red + hu * 16384 + 1024) = f32x4{cc[r0 + 4], cc[r0 + 5], cc[r0 + 6], cc[r0 + 7]};
        }
        __syncthreads();
        const char* rd = smem + w * 16384 + l16;
        f32x4 r0 = *(const f32x4*)(rd), r1 = *(const f32x4*)(rd + 1024);
#pragma unroll
        for (int s7 = 1; s7 < 8; s7++) { r0 += *(const f32x4*)(rd + s7 * 2048); r1 += *(const f32x4*)(rd + s7 * 2048 + 1024); }
        u16* orow = p.out + (size_t)(umt * 32 + c) * D_ + unt * 32 + 16 * us + 4 * h;
        u32x2 o0, o1;
        o0[0] = pk2v(fma2(f32x2{gbv[0][0], gbv[0][1]}, f32x2{r0[0], r0[1]}, up2(x1p[0])));
        o0[1] = pk2v(fma2(f32x2{gbv[0][2], gbv[0][3]}, f32x2{r0[2], r0[3]}, up2(x1p[1])));
        o1[0] = pk2v(fma2(f32x2{gbv[1][0], gbv[1][1]}, f32x2{r1[0], r1[1]}, up2(x1p[2])));
        o1[1] = pk2v(fma2(f32x2{gbv[1][2], gbv[1][3]}, f32x2{r1[2], r1[3]}, up2(x1p[3])));
        *(u32x2*)(orow) = o0;
        *(u32x2*)(orow + 8) = o1;
    }
}

extern "C" void kernel_launch(void* const* d_in, const int* in_sizes, int n_in,
                              void* d_out, int out_size, void* d_ws, size_t ws_size,
                              hipStream_t stream) {
    Params p;
    p.x = (const u16*)d_in[0]; p.wqkv = (const u16*)d_in[1]; p.wo = (const u16*)d_in[2];
    p.w1 = (const u16*)d_in[4]; p.w2 = (const u16*)d_in[5]; p.c = (const u16*)d_in[6]; p.wmod = (const u16*)d_in[7];
    p.out = (u16*)d_out; p.ws = (char*)d_ws;
    k_main<<<256, 512, 0, stream>>>(p);
}
```
